```python
import torch

def _softmax(x, axis=-1):
    return torch.softmax(x, dim=axis)
def _gelu(x):
    return torch.nn.functional.gelu(x, approximate="tanh")
def _silu(x):
    return torch.nn.functional.silu(x)
def _sigmoid(x):
    return torch.sigmoid(x)
def _layernorm(x, axis=-1, eps=1e-5):
    m = x.mean(dim=axis, keepdim=True)
    v = x.var(dim=axis, keepdim=True, unbiased=False)
    return (x - m) / torch.sqrt(v + eps)
def _scatter_add(tgt, idx, src, axis=0):
    return tgt.clone().index_add_(axis, idx, src)
def _segment_sum(src, seg, n):
    out = torch.zeros((n,) + src.shape[1:], dtype=src.dtype)
    return out.index_add_(0, seg, src)
def _pad(x, pw):
    flat = tuple(v for pair in reversed(pw) for v in pair)
    return torch.nn.functional.pad(x, flat)

kernel_name = 'recurrent_scan__gqa_block_cached__mixer_channel_mlp__decode'

def setup_inputs():

    _w = (lambda a: (lambda t: t.to(torch.bfloat16) if t.is_floating_point() else t)(torch.as_tensor(a)))
    def _A_setup():
        B, Sq, D = 2, 1, 512
        Hq, Hkv, Sk, Dh = 32, 8, 32768, 128
        assert Hq % Hkv == 0, (Hq, Hkv)
        return {
            "x": _w((lambda *s: torch.randn(*s, dtype=torch.bfloat16) * 0.1)(B, Sq, D)),
            "k_cache": _w((lambda *s: torch.randn(*s, dtype=torch.bfloat16) * 0.8)(B, Hkv, Sk, Dh)),
            "v_cache": _w((lambda *s: torch.randn(*s, dtype=torch.bfloat16) * 3.2)(B, Hkv, Sk, Dh)),
            "wq": _w((lambda *s: torch.randn(*s, dtype=torch.bfloat16) * 0.8)(D, Hq * Dh) * (D ** -0.5)),
            "wo": _w((lambda *s: torch.randn(*s, dtype=torch.bfloat16) * 3.2)(Hq * Dh, D) * ((Hq * Dh) ** -0.5)),
        }
    _A_in = _A_setup()

    def _B_setup():
        B, S, D, F = 2, 1, 512, 1792
        return {
            "x": _w((lambda *s: torch.randn(*s, dtype=torch.bfloat16) * 0.1)(B, S, D)),
            "w1": _w((lambda *s: torch.randn(*s, dtype=torch.bfloat16) * 3.2)(D, F) * (D ** -0.5)),
            "b1": _w((lambda *s: torch.randn(*s, dtype=torch.bfloat16) * 3.2)(F)),
            "w2": _w((lambda *s: torch.randn(*s, dtype=torch.bfloat16) * 3.2)(F, D) * (F ** -0.5)),
            "b2": _w((lambda *s: torch.randn(*s, dtype=torch.bfloat16) * 3.2)(D)),
        }
    _B_in = _B_setup()

    return {
        "A_x": _A_in["x"],
        "A_k_cache": _A_in["k_cache"],
        "A_v_cache": _A_in["v_cache"],
        "A_wq": _A_in["wq"],
        "A_wo": _A_in["wo"],
        "B_x": _B_in["x"],
        "B_w1": _B_in["w1"],
        "B_b1": _B_in["b1"],
        "B_w2": _B_in["w2"],
        "B_b2": _B_in["b2"],
    }

def reference(A_x, A_k_cache, A_v_cache, A_wq, A_wo, B_x, B_w1, B_b1, B_w2, B_b2):
    def _A_ref(x, k_cache, v_cache, wq, wo):
        B, Sq, D = x.shape
        Hq, Dh = 32, 128
        Hkv = k_cache.shape[1]
        rep = Hq // Hkv
        q = (lambda x, *d: x.permute(*d))((x @ wq).reshape(B, Sq, Hq, Dh), 0, 2, 1, 3)
        k = (lambda x, n, axis: torch.repeat_interleave(x, n, dim=axis))(k_cache, rep, axis=1)
        v = (lambda x, n, axis: torch.repeat_interleave(x, n, dim=axis))(v_cache, rep, axis=1)
        scores = torch.einsum("bhqd,bhkd->bhqk", q, k) * (Dh ** -0.5)
        p = _softmax(scores, axis=-1)
        o = torch.einsum("bhqk,bhkd->bhqd", p, v)
        return (lambda x, *d: x.permute(*d))(o, 0, 2, 1, 3).reshape(B, Sq, Hq * Dh) @ wo

    def _B_ref(x, w1, b1, w2, b2):
        y = _layernorm(x, axis=-1)
        h = _gelu(torch.einsum("bsd,df->bsf", y, w1) + b1)
        return torch.einsum("bsf,fd->bsd", h, w2) + b2

    x = A_x
    xn = _layernorm(x, axis=-1)
    s = _A_ref(xn, A_k_cache, A_v_cache, A_wq, A_wo)
    g = _silu(_B_ref(xn, B_w1, B_b1, B_w2, B_b2))
    return x + s * g
```

```cpp
#include <hip/hip_runtime.h>
#include <hip/hip_bf16.h>
#include <stdint.h>

typedef unsigned short u16;
typedef unsigned u32x4 __attribute__((ext_vector_type(4)));
typedef float f32x4 __attribute__((ext_vector_type(4)));

constexpr int B = 2, D = 512, HQ = 32, HKV = 8, SK = 32768, DH = 128, F = 1792;
constexpr int NSPLIT = 16;

__device__ __forceinline__ float bf2f(u16 x) { return __uint_as_float(((unsigned)x) << 16); }
__device__ __forceinline__ float rbf(float f) { unsigned u = __float_as_uint(f); u += 0x7FFFu + ((u >> 16) & 1u); return __uint_as_float(u & 0xFFFF0000u); }
__device__ __forceinline__ u16 f2bf(float f) { unsigned u = __float_as_uint(f); u += 0x7FFFu + ((u >> 16) & 1u); return (u16)(u >> 16); }

__device__ void ln_block(const float* in, float* out, float* red) {
  int t = threadIdx.x;
  float a = in[t], b = in[t + 256];
  float s = a + b;
  for (int m = 32; m >= 1; m >>= 1) s += __shfl_xor(s, m);
  if ((t & 63) == 0) red[t >> 6] = s;
  __syncthreads();
  float mean = (red[0] + red[1] + red[2] + red[3]) * (1.f / 512.f);
  __syncthreads();
  float da = a - mean, db = b - mean;
  float v = da * da + db * db;
  for (int m = 32; m >= 1; m >>= 1) v += __shfl_xor(v, m);
  if ((t & 63) == 0) red[t >> 6] = v;
  __syncthreads();
  float var = (red[0] + red[1] + red[2] + red[3]) * (1.f / 512.f);
  __syncthreads();
  float mb = rbf(mean), vb = rbf(var);
  float den = rbf(sqrtf(rbf(vb + 1e-5f)));
  out[t] = rbf(rbf(a - mb) / den);
  out[t + 256] = rbf(rbf(b - mb) / den);
  __syncthreads();
}

__global__ void __launch_bounds__(256) k_pre(const u16* x, const u16* wq, const u16* w1, const u16* b1, float* q, float* h) {
  __shared__ float xs[2][512], xn[2][512], y[2][512], red[4], part[4][2][64];
  int t = threadIdx.x;
  for (int i = t; i < 1024; i += 256) xs[i >> 9][i & 511] = bf2f(x[i]);
  __syncthreads();
  ln_block(xs[0], xn[0], red); ln_block(xs[1], xn[1], red);
  bool isq = blockIdx.x < 64;
  if (!isq) { ln_block(xn[0], y[0], red); ln_block(xn[1], y[1], red); }
  int c = t & 63, kq = t >> 6;
  int col = isq ? blockIdx.x * 64 + c : (blockIdx.x - 64) * 64 + c;
  const u16* w = isq ? wq : w1; int ld = isq ? 4096 : F;
  const float* a0 = isq ? xn[0] : y[0]; const float* a1 = isq ? xn[1] : y[1];
  float s0 = 0, s1 = 0;
  for (int k = kq * 128; k < kq * 128 + 128; ++k) { float wv = bf2f(w[(size_t)k * ld + col]); s0 += a0[k] * wv; s1 += a1[k] * wv; }
  part[kq][0][c] = s0; part[kq][1][c] = s1;
  __syncthreads();
  if (t < 128) {
    int b = t >> 6;
    float s = part[0][b][c] + part[1][b][c] + part[2][b][c] + part[3][b][c];
    if (isq) q[b * 4096 + col] = rbf(s);
    else { float z = rbf(rbf(s) + bf2f(b1[col])); float g = 0.5f * z * (1.f + tanhf(0.7978845608028654f * (z + 0.044715f * z * z * z))); h[b * F + col] = rbf(g); }
  }
}

__global__ void __launch_bounds__(256) k_attn(const u16* K, const u16* V, const float* q, float* part) {
  __shared__ float qs[4][128], ps[4][256], red[4][4], accs[4][4][128];
  int blk = blockIdx.x, kvh = blk & 7, b = (blk >> 3) & 1, s = blk >> 4;
  int t = threadIdx.x, lane = t & 63, w = t >> 6;
  for (int i = t; i < 512; i += 256) qs[i >> 7][i & 127] = q[b * 4096 + kvh * 512 + i];
  __syncthreads();
  const u16* Kb = K + ((size_t)(b * HKV + kvh) * SK + s * 2048) * DH;
  const u16* Vb = V + ((size_t)(b * HKV + kvh) * SK + s * 2048) * DH;
  float m[4] = {-INFINITY, -INFINITY, -INFINITY, -INFINITY}, l[4] = {0, 0, 0, 0};
  float acc[4][2] = {};
  for (int t0 = 0; t0 < 2048; t0 += 256) {
    const u32x4* kr = (const u32x4*)(Kb + (size_t)(t0 + t) * DH);
    float sc[4] = {0, 0, 0, 0};
    for (int c = 0; c < 16; ++c) { u32x4 kv = kr[c];
      for (int j = 0; j < 4; ++j) { float lo = __uint_as_float(kv[j] << 16), hi = __uint_as_float(kv[j] & 0xffff0000u);
        for (int hh = 0; hh < 4; ++hh) sc[hh] += lo * qs[hh][c * 8 + 2 * j] + hi * qs[hh][c * 8 + 2 * j + 1]; } }
    float mx[4];
    for (int hh = 0; hh < 4; ++hh) { sc[hh] *= 0.08838834764831845f; float v = sc[hh]; for (int o = 32; o >= 1; o >>= 1) v = fmaxf(v, __shfl_xor(v, o)); if (lane == 0) red[hh][w] = v; }
    __syncthreads();
    for (int hh = 0; hh < 4; ++hh) mx[hh] = fmaxf(fmaxf(fmaxf(red[hh][0], red[hh][1]), fmaxf(red[hh][2], red[hh][3])), m[hh]);
    for (int hh = 0; hh < 4; ++hh) { float p = __expf(sc[hh] - mx[hh]); ps[hh][t] = p; float a = __expf(m[hh] - mx[hh]); l[hh] = l[hh] * a + p; acc[hh][0] *= a; acc[hh][1] *= a; m[hh] = mx[hh]; }
    __syncthreads();
    for (int k = w * 64; k < w * 64 + 64; ++k) { unsigned vv = *(const unsigned*)(Vb + (size_t)(t0 + k) * DH + 2 * lane);
      float lo = __uint_as_float(vv << 16), hi = __uint_as_float(vv & 0xffff0000u);
      for (int hh = 0; hh < 4; ++hh) { acc[hh][0] += ps[hh][k] * lo; acc[hh][1] += ps[hh][k] * hi; } }
    __syncthreads();
  }
  for (int hh = 0; hh < 4; ++hh) { accs[w][hh][2 * lane] = acc[hh][0]; accs[w][hh][2 * lane + 1] = acc[hh][1]; float v = l[hh]; for (int o = 32; o >= 1; o >>= 1) v += __shfl_xor(v, o); if (lane == 0) red[hh][w] = v; }
  __syncthreads();
  float* po = part + ((size_t)(b * HQ + kvh * 4) * NSPLIT) * 130;
  for (int i = t; i < 512; i += 256) { int hh = i >> 7, d = i & 127; po[(hh * NSPLIT + s) * 130 + 2 + d] = accs[0][hh][d] + accs[1][hh][d] + accs[2][hh][d] + accs[3][hh][d]; }
  if (t < 4) { po[(t * NSPLIT + s) * 130] = m[t]; po[(t * NSPLIT + s) * 130 + 1] = red[t][0] + red[t][1] + red[t][2] + red[t][3]; }
}

__global__ void __launch_bounds__(256) k_post(const u16* x, const float* part, const u16* wo, const float* h, const u16* w2, const u16* b2, u16* out) {
  __shared__ float o[2][4096], hs[2][F], red[16][2][2][16];
  int t = threadIdx.x;
  for (int i = t; i < 8192; i += 256) { int b = i >> 12, f = i & 4095, hh = f >> 7, d = f & 127;
    const float* p = part + (size_t)(b * HQ + hh) * NSPLIT * 130; float M = -INFINITY; for (int s = 0; s < NSPLIT; ++s) M = fmaxf(M, p[s * 130]);
    float L = 0, a = 0; for (int s = 0; s < NSPLIT; ++s) { float wgt = __expf(p[s * 130] - M); L += wgt * p[s * 130 + 1]; a += wgt * p[s * 130 + 2 + d]; }
    o[b][f] = rbf(a / L); }
  for (int i = t; i < 2 * F; i += 256) hs[i / F][i % F] = h[i];
  __syncthreads();
  int c = t & 15, kq = t >> 4, col = blockIdx.x * 16 + c;
  float s0 = 0, s1 = 0, g0 = 0, g1 = 0;
  for (int k = kq * 256; k < kq * 256 + 256; ++k) { float wv = bf2f(wo[(size_t)k * D + col]); s0 += o[0][k] * wv; s1 += o[1][k] * wv; }
  for (int k = kq * 112; k < kq * 112 + 112; ++k) { float wv = bf2f(w2[(size_t)k * D + col]); g0 += hs[0][k] * wv; g1 += hs[1][k] * wv; }
  red[kq][0][0][c] = s0; red[kq][0][1][c] = s1; red[kq][1][0][c] = g0; red[kq][1][1][c] = g1;
  __syncthreads();
  if (t < 32) { int b = t >> 4; float s = 0, g = 0; for (int k = 0; k < 16; ++k) { s += red[k][0][b][c]; g += red[k][1][b][c]; }
    s = rbf(s); g = rbf(rbf(g) + bf2f(b2[col])); g = rbf(g / (1.f + __expf(-g)));
    out[b * D + col] = f2bf(bf2f(x[b * D + col]) + rbf(s * g)); }
}

extern "C" void kernel_launch(void* const* d_in, const int* in_sizes, int n_in, void* d_out, int out_size, void* d_ws, size_t ws_size, hipStream_t stream) {
  const u16* x = (const u16*)d_in[0]; const u16* K = (const u16*)d_in[1]; const u16* V = (const u16*)d_in[2];
  const u16* wq = (const u16*)d_in[3]; const u16* wo = (const u16*)d_in[4];
  const u16* w1 = (const u16*)d_in[6]; const u16* b1 = (const u16*)d_in[7]; const u16* w2 = (const u16*)d_in[8]; const u16* b2 = (const u16*)d_in[9];
  float* q = (float*)d_ws; float* h = q + 8192; float* part = h + 4096;
  k_pre<<<64 + 28, 256, 0, stream>>>(x, wq, w1, b1, q, h);
  k_attn<<<256, 256, 0, stream>>>(K, V, q, part);
  k_post<<<32, 256, 0, stream>>>(x, part, wo, h, w2, b2, (u16*)d_out);
}
```

```cpp
#define AUXKV 2
#include <hip/hip_runtime.h>
#include <hip/hip_bf16.h>
#include <stdint.h>

typedef unsigned short u16;
typedef unsigned u32x4 __attribute__((ext_vector_type(4)));
typedef float f32x4 __attribute__((ext_vector_type(4)));

constexpr int B = 2, D = 512, HQ = 32, HKV = 8, SK = 32768, DH = 128, F = 1792;
constexpr int NSPLIT = 16;

__device__ __forceinline__ float bf2f(u16 x) { return __uint_as_float(((unsigned)x) << 16); }
__device__ __forceinline__ float rbf(float f) { unsigned u = __float_as_uint(f); u += 0x7FFFu + ((u >> 16) & 1u); return __uint_as_float(u & 0xFFFF0000u); }
__device__ __forceinline__ u16 f2bf(float f) { unsigned u = __float_as_uint(f); u += 0x7FFFu + ((u >> 16) & 1u); return (u16)(u >> 16); }

__device__ void ln_block(const float* in, float* out, float* red) {
  int t = threadIdx.x;
  float a = in[t], b = in[t + 256];
  float s = a + b;
  for (int m = 32; m >= 1; m >>= 1) s += __shfl_xor(s, m);
  if ((t & 63) == 0) red[t >> 6] = s;
  __syncthreads();
  float mean = (red[0] + red[1] + red[2] + red[3]) * (1.f / 512.f);
  __syncthreads();
  float da = a - mean, db = b - mean;
  float v = da * da + db * db;
  for (int m = 32; m >= 1; m >>= 1) v += __shfl_xor(v, m);
  if ((t & 63) == 0) red[t >> 6] = v;
  __syncthreads();
  float var = (red[0] + red[1] + red[2] + red[3]) * (1.f / 512.f);
  __syncthreads();
  float mb = rbf(mean), vb = rbf(var);
  float den = rbf(sqrtf(rbf(vb + 1e-5f)));
  out[t] = rbf(rbf(a - mb) / den);
  out[t + 256] = rbf(rbf(b - mb) / den);
  __syncthreads();
}

__global__ void __launch_bounds__(256) k_pre(const u16* x, const u16* wq, const u16* w1, const u16* b1, float* q, float* h) {
  __shared__ float xs[2][512], xn[2][512], y[2][512], red[4], part[4][2][64];
  int t = threadIdx.x;
  for (int i = t; i < 1024; i += 256) xs[i >> 9][i & 511] = bf2f(x[i]);
  __syncthreads();
  ln_block(xs[0], xn[0], red); ln_block(xs[1], xn[1], red);
  bool isq = blockIdx.x < 64;
  if (!isq) { ln_block(xn[0], y[0], red); ln_block(xn[1], y[1], red); }
  int c = t & 63, kq = t >> 6;
  int col = isq ? blockIdx.x * 64 + c : (blockIdx.x - 64) * 64 + c;
  const u16* w = isq ? wq : w1; int ld = isq ? 4096 : F;
  const float* a0 = isq ? xn[0] : y[0]; const float* a1 = isq ? xn[1] : y[1];
  float s0 = 0, s1 = 0;
  for (int k = kq * 128; k < kq * 128 + 128; ++k) { float wv = bf2f(w[(size_t)k * ld + col]); s0 += a0[k] * wv; s1 += a1[k] * wv; }
  part[kq][0][c] = s0; part[kq][1][c] = s1;
  __syncthreads();
  if (t < 128) {
    int b = t >> 6;
    float s = part[0][b][c] + part[1][b][c] + part[2][b][c] + part[3][b][c];
    if (isq) q[b * 4096 + col] = rbf(s);
    else { float z = rbf(rbf(s) + bf2f(b1[col])); float g = 0.5f * z * (1.f + tanhf(0.7978845608028654f * (z + 0.044715f * z * z * z))); h[b * F + col] = rbf(g); }
  }
}

typedef short bf16x8 __attribute__((ext_vector_type(8)));
__device__ __forceinline__ f32x4 mfma16(u32x4 a, u32x4 b, f32x4 c) {
  return __builtin_amdgcn_mfma_f32_16x16x32_bf16(__builtin_bit_cast(bf16x8, a), __builtin_bit_cast(bf16x8, b), c, 0, 0, 0);
}
typedef __bf16 bf16x2_t __attribute__((ext_vector_type(2)));
typedef float f32x2 __attribute__((ext_vector_type(2)));
__device__ __forceinline__ unsigned cvtpk(float lo, float hi) { return __builtin_bit_cast(unsigned, __builtin_convertvector(f32x2{lo, hi}, bf16x2_t)); }
__device__ __forceinline__ float xmax(float x) {
  auto s = __builtin_amdgcn_permlane16_swap(__float_as_uint(x), __float_as_uint(x), false, false);
  x = fmaxf(__uint_as_float(s[0]), __uint_as_float(s[1]));
  auto t = __builtin_amdgcn_permlane32_swap(__float_as_uint(x), __float_as_uint(x), false, false);
  return fmaxf(__uint_as_float(t[0]), __uint_as_float(t[1]));
}
__device__ __forceinline__ float xsum(float x) {
  auto s = __builtin_amdgcn_permlane16_swap(__float_as_uint(x), __float_as_uint(x), false, false);
  x = __uint_as_float(s[0]) + __uint_as_float(s[1]);
  auto t = __builtin_amdgcn_permlane32_swap(__float_as_uint(x), __float_as_uint(x), false, false);
  return __uint_as_float(t[0]) + __uint_as_float(t[1]);
}
constexpr int NW = 8;
constexpr int NT = 2048 / 32 / NW;
__global__ void __launch_bounds__(NW * 64) k_attn(const u16* K, const u16* V, const float* q, float* part) {
  __shared__ float mrg[NW][4][132];
  const int blk = blockIdx.x, kvh = blk & 7, b = (blk >> 3) & 1, s = blk >> 4;
  const int lane = threadIdx.x & 63, wave = __builtin_amdgcn_readfirstlane(threadIdx.x >> 6);
  const int g = lane >> 4, r = lane & 15;
  auto rk = __builtin_amdgcn_make_buffer_rsrc((void*)K, 0, 128u << 20, 0x00020000);
  auto rv = __builtin_amdgcn_make_buffer_rsrc((void*)V, 0, 128u << 20, 0x00020000);
  const unsigned base = ((b * HKV + kvh) * SK + s * 2048 + wave * 32) * 256u;
  const unsigned koff = r * 256 + g * 16, voff = g * 1024 + r * 16;
  u32x4 kb[2][8], vb[2][8];
#define ISSUE(S, T) do { const unsigned o_ = base + (unsigned)(T) * (NW * 8192u); \
    _Pragma("unroll") for (int i = 0; i < 8; ++i) kb[S][i] = __builtin_amdgcn_raw_buffer_load_b128(rk, koff + (i & 3) * 64 + (i >> 2) * 4096, o_, AUXKV); \
    _Pragma("unroll") for (int i = 0; i < 8; ++i) vb[S][i] = __builtin_amdgcn_raw_buffer_load_b128(rv, voff + (i & 3) * 256 + (i >> 2) * 4096, o_, AUXKV); \
    __builtin_amdgcn_sched_barrier(0); } while (0)
  u32x4 qf[4];
  { const float* qp = q + b * 4096 + (kvh * 4 + (r & 3)) * 128 + g * 8;
#pragma unroll
    for (int t = 0; t < 4; ++t) { f32x4 a = *(const f32x4*)(qp + t * 32), c = *(const f32x4*)(qp + t * 32 + 4);
      qf[t] = u32x4{cvtpk(a[0], a[1]), cvtpk(a[2], a[3]), cvtpk(c[0], c[1]), cvtpk(c[2], c[3])}; } }
  __builtin_amdgcn_sched_barrier(0);
  ISSUE(0, 0); ISSUE(1, 1);
  constexpr float C2 = 0.08838834764831845f * 1.4426950408889634f;
  float m = -INFINITY, l = 0.f;
  f32x4 acc[8];
#pragma unroll
  for (int e = 0; e < 8; ++e) acc[e] = f32x4{0, 0, 0, 0};
#define TILE(S) do { \
    f32x4 d0 = {0,0,0,0}, d1 = {0,0,0,0}; \
    _Pragma("unroll") for (int t = 0; t < 4; ++t) { d0 = mfma16(kb[S][t], qf[t], d0); d1 = mfma16(kb[S][4 + t], qf[t], d1); } \
    float tm = fmaxf(fmaxf(fmaxf(d0[0], d0[1]), fmaxf(d0[2], d0[3])), fmaxf(fmaxf(d1[0], d1[1]), fmaxf(d1[2], d1[3]))); \
    tm = xmax(tm); \
    const float mn = fmaxf(m, tm); \
    if (__builtin_amdgcn_ballot_w64(mn != m) != 0) { const float al = __builtin_amdgcn_exp2f((m - mn) * C2); l *= al; \
      _Pragma("unroll") for (int e = 0; e < 8; ++e) acc[e] *= al; m = mn; } \
    const float mc = m * C2; float p[8]; \
    _Pragma("unroll") for (int i = 0; i < 4; ++i) { p[i] = __builtin_amdgcn_exp2f(fmaf(d0[i], C2, -mc)); p[4 + i] = __builtin_amdgcn_exp2f(fmaf(d1[i], C2, -mc)); } \
    l += ((p[0] + p[1]) + (p[2] + p[3])) + ((p[4] + p[5]) + (p[6] + p[7])); \
    const u32x4 pf = {cvtpk(p[0], p[1]), cvtpk(p[2], p[3]), cvtpk(p[4], p[5]), cvtpk(p[6], p[7])}; \
    _Pragma("unroll") for (int w = 0; w < 4; ++w) { \
      u32x4 lo, hi; \
      _Pragma("unroll") for (int jp = 0; jp < 4; ++jp) { lo[jp] = __builtin_amdgcn_perm(vb[S][2 * jp + 1][w], vb[S][2 * jp][w], 0x05040100u); hi[jp] = __builtin_amdgcn_perm(vb[S][2 * jp + 1][w], vb[S][2 * jp][w], 0x07060302u); } \
      acc[2 * w] = mfma16(lo, pf, acc[2 * w]); acc[2 * w + 1] = mfma16(hi, pf, acc[2 * w + 1]); } \
    __builtin_amdgcn_sched_barrier(0); } while (0)
  for (int t = 0; t < NT - 2; t += 2) {
    TILE(0); ISSUE(0, t + 2);
    TILE(1); ISSUE(1, t + 3);
  }
  TILE(0); TILE(1);
  l = xsum(l);
  if (r < 4) {
#pragma unroll
    for (int i = 0; i < 4; ++i) {
      *(f32x4*)&mrg[wave][r][4 + 8 * (4 * g + i)] = f32x4{acc[0][i], acc[1][i], acc[2][i], acc[3][i]};
      *(f32x4*)&mrg[wave][r][8 + 8 * (4 * g + i)] = f32x4{acc[4][i], acc[5][i], acc[6][i], acc[7][i]};
    }
    if (g == 0) { mrg[wave][r][0] = m; mrg[wave][r][1] = l; }
  }
  __syncthreads();
  { const int hh = threadIdx.x >> 7, d = threadIdx.x & 127;
    float M = mrg[0][hh][0];
#pragma unroll
    for (int w = 1; w < NW; ++w) M = fmaxf(M, mrg[w][hh][0]);
    float L = 0, o = 0;
#pragma unroll
    for (int w = 0; w < NW; ++w) { const float wt = __builtin_amdgcn_exp2f((mrg[w][hh][0] - M) * C2); L += wt * mrg[w][hh][1]; o += wt * mrg[w][hh][4 + d]; }
    float* po = part + ((size_t)(b * HQ + kvh * 4 + hh) * NSPLIT + s) * 130;
    po[2 + d] = o; if (d == 0) { po[0] = M * 0.08838834764831845f; po[1] = L; } }
}

__global__ void __launch_bounds__(256) k_post(const u16* x, const float* part, const u16* wo, const float* h, const u16* w2, const u16* b2, u16* out) {
  __shared__ float o[2][4096], hs[2][F], red[16][2][2][16];
  int t = threadIdx.x;
  for (int i = t; i < 8192; i += 256) { int b = i >> 12, f = i & 4095, hh = f >> 7, d = f & 127;
    const float* p = part + (size_t)(b * HQ + hh) * NSPLIT * 130; float M = -INFINITY; for (int s = 0; s < NSPLIT; ++s) M = fmaxf(M, p[s * 130]);
    float L = 0, a = 0; for (int s = 0; s < NSPLIT; ++s) { float wgt = __expf(p[s * 130] - M); L += wgt * p[s * 130 + 1]; a += wgt * p[s * 130 + 2 + d]; }
    o[b][f] = rbf(a / L); }
  for (int i = t; i < 2 * F; i += 256) hs[i / F][i % F] = h[i];
  __syncthreads();
  int c = t & 15, kq = t >> 4, col = blockIdx.x * 16 + c;
  float s0 = 0, s1 = 0, g0 = 0, g1 = 0;
  for (int k = kq * 256; k < kq * 256 + 256; ++k) { float wv = bf2f(wo[(size_t)k * D + col]); s0 += o[0][k] * wv; s1 += o[1][k] * wv; }
  for (int k = kq * 112; k < kq * 112 + 112; ++k) { float wv = bf2f(w2[(size_t)k * D + col]); g0 += hs[0][k] * wv; g1 += hs[1][k] * wv; }
  red[kq][0][0][c] = s0; red[kq][0][1][c] = s1; red[kq][1][0][c] = g0; red[kq][1][1][c] = g1;
  __syncthreads();
  if (t < 32) { int b = t >> 4; float s = 0, g = 0; for (int k = 0; k < 16; ++k) { s += red[k][0][b][c]; g += red[k][1][b][c]; }
    s = rbf(s); g = rbf(rbf(g) + bf2f(b2[col])); g = rbf(g / (1.f + __expf(-g)));
    out[b * D + col] = f2bf(bf2f(x[b * D + col]) + rbf(s * g)); }
}

extern "C" void kernel_launch(void* const* d_in, const int* in_sizes, int n_in, void* d_out, int out_size, void* d_ws, size_t ws_size, hipStream_t stream) {
  const u16* x = (const u16*)d_in[0]; const u16* K = (const u16*)d_in[1]; const u16* V = (const u16*)d_in[2];
  const u16* wq = (const u16*)d_in[3]; const u16* wo = (const u16*)d_in[4];
  const u16* w1 = (const u16*)d_in[6]; const u16* b1 = (const u16*)d_in[7]; const u16* w2 = (const u16*)d_in[8]; const u16* b2 = (const u16*)d_in[9];
  float* q = (float*)d_ws; float* h = q + 8192; float* part = h + 4096;
  k_pre<<<64 + 28, 256, 0, stream>>>(x, wq, w1, b1, q, h);
  k_attn<<<256, NW * 64, 0, stream>>>(K, V, q, part);
  k_post<<<32, 256, 0, stream>>>(x, part, wo, h, w2, b2, (u16*)d_out);
}
```

```cpp
#define AUXKV 18
#include <hip/hip_runtime.h>
#include <hip/hip_bf16.h>
#include <stdint.h>

typedef unsigned short u16;
typedef unsigned u32x4 __attribute__((ext_vector_type(4)));
typedef float f32x4 __attribute__((ext_vector_type(4)));

constexpr int B = 2, D = 512, HQ = 32, HKV = 8, SK = 32768, DH = 128, F = 1792;
constexpr int NSPLIT = 16;

__device__ __forceinline__ float bf2f(u16 x) { return __uint_as_float(((unsigned)x) << 16); }
__device__ __forceinline__ float rbf(float f) { unsigned u = __float_as_uint(f); u += 0x7FFFu + ((u >> 16) & 1u); return __uint_as_float(u & 0xFFFF0000u); }
__device__ __forceinline__ u16 f2bf(float f) { unsigned u = __float_as_uint(f); u += 0x7FFFu + ((u >> 16) & 1u); return (u16)(u >> 16); }

__device__ void ln_block(const float* in, float* out, float* red) {
  int t = threadIdx.x;
  float a = in[t], b = in[t + 256];
  float s = a + b;
  for (int m = 32; m >= 1; m >>= 1) s += __shfl_xor(s, m);
  if ((t & 63) == 0) red[t >> 6] = s;
  __syncthreads();
  float mean = (red[0] + red[1] + red[2] + red[3]) * (1.f / 512.f);
  __syncthreads();
  float da = a - mean, db = b - mean;
  float v = da * da + db * db;
  for (int m = 32; m >= 1; m >>= 1) v += __shfl_xor(v, m);
  if ((t & 63) == 0) red[t >> 6] = v;
  __syncthreads();
  float var = (red[0] + red[1] + red[2] + red[3]) * (1.f / 512.f);
  __syncthreads();
  float mb = rbf(mean), vb = rbf(var);
  float den = rbf(sqrtf(rbf(vb + 1e-5f)));
  out[t] = rbf(rbf(a - mb) / den);
  out[t + 256] = rbf(rbf(b - mb) / den);
  __syncthreads();
}

__global__ void __launch_bounds__(256) k_pre(const u16* x, const u16* wq, const u16* w1, const u16* b1, float* q, float* h) {
  __shared__ float xs[2][512], xn[2][512], y[2][512], red[4], part[16][257];
  int t = threadIdx.x;
  const bool isq = blockIdx.x < 256;
  const int bi = isq ? (blockIdx.x & 7) * 32 + (blockIdx.x >> 3) : blockIdx.x - 256;
  const u16* w = isq ? wq : w1; const int ld = isq ? 4096 : F;
  const int half = t & 1, rg = t >> 1;
  u32x4 wv[4];
  for (int i = 0; i < 4; ++i) wv[i] = *(const u32x4*)(w + (size_t)(rg + 128 * i) * ld + bi * 16 + half * 8);
  for (int i = t; i < 1024; i += 256) xs[i >> 9][i & 511] = bf2f(x[i]);
  __syncthreads();
  ln_block(xs[0], xn[0], red); ln_block(xs[1], xn[1], red);
  if (!isq) { ln_block(xn[0], y[0], red); ln_block(xn[1], y[1], red); }
  const float* a0 = isq ? xn[0] : y[0]; const float* a1 = isq ? xn[1] : y[1];
  float acc[16] = {};
  for (int i = 0; i < 4; ++i) { float x0 = a0[rg + 128 * i], x1 = a1[rg + 128 * i];
    for (int j = 0; j < 4; ++j) { float lo = __uint_as_float(wv[i][j] << 16), hi = __uint_as_float(wv[i][j] & 0xffff0000u);
      acc[2 * j] += x0 * lo; acc[2 * j + 1] += x0 * hi; acc[8 + 2 * j] += x1 * lo; acc[8 + 2 * j + 1] += x1 * hi; } }
  for (int j = 0; j < 16; ++j) part[j][t] = acc[j];
  __syncthreads();
  { const int o = t >> 3, sub = t & 7;
    const int hf = o >> 4, j = o & 15; float sum = 0;
    for (int i = 0; i < 16; ++i) sum += part[j][2 * (sub * 16 + i) + hf];
    sum += __shfl_xor(sum, 1); sum += __shfl_xor(sum, 2); sum += __shfl_xor(sum, 4);
    if (sub == 0) { const int b = j >> 3, col = bi * 16 + hf * 8 + (j & 7);
      if (isq) q[b * 4096 + col] = rbf(sum);
      else { float z = rbf(rbf(sum) + bf2f(b1[col])); float g = 0.5f * z * (1.f + tanhf(0.7978845608028654f * (z + 0.044715f * z * z * z))); h[b * F + col] = rbf(g); } } }
}

typedef short bf16x8 __attribute__((ext_vector_type(8)));
__device__ __forceinline__ f32x4 mfma16(u32x4 a, u32x4 b, f32x4 c) {
  return __builtin_amdgcn_mfma_f32_16x16x32_bf16(__builtin_bit_cast(bf16x8, a), __builtin_bit_cast(bf16x8, b), c, 0, 0, 0);
}
typedef __bf16 bf16x2_t __attribute__((ext_vector_type(2)));
typedef float f32x2 __attribute__((ext_vector_type(2)));
__device__ __forceinline__ unsigned cvtpk(float lo, float hi) { return __builtin_bit_cast(unsigned, __builtin_convertvector(f32x2{lo, hi}, bf16x2_t)); }
__device__ __forceinline__ float xmax(float x) {
  auto s = __builtin_amdgcn_permlane16_swap(__float_as_uint(x), __float_as_uint(x), false, false);
  x = fmaxf(__uint_as_float(s[0]), __uint_as_float(s[1]));
  auto t = __builtin_amdgcn_permlane32_swap(__float_as_uint(x), __float_as_uint(x), false, false);
  return fmaxf(__uint_as_float(t[0]), __uint_as_float(t[1]));
}
__device__ __forceinline__ float xsum(float x) {
  auto s = __builtin_amdgcn_permlane16_swap(__float_as_uint(x), __float_as_uint(x), false, false);
  x = __uint_as_float(s[0]) + __uint_as_float(s[1]);
  auto t = __builtin_amdgcn_permlane32_swap(__float_as_uint(x), __float_as_uint(x), false, false);
  return __uint_as_float(t[0]) + __uint_as_float(t[1]);
}
constexpr int NW = 8;
constexpr int NT = 2048 / 32 / NW;
__global__ void __launch_bounds__(NW * 64) k_attn(const u16* K, const u16* V, const float* q, float* part) {
  __shared__ float mrg[NW][4][132];
  const int blk = blockIdx.x, kvh = blk & 7, b = (blk >> 3) & 1, s = blk >> 4;
  const int lane = threadIdx.x & 63, wave = __builtin_amdgcn_readfirstlane(threadIdx.x >> 6);
  const int g = lane >> 4, r = lane & 15;
  auto rk = __builtin_amdgcn_make_buffer_rsrc((void*)K, 0, 128u << 20, 0x00020000);
  auto rv = __builtin_amdgcn_make_buffer_rsrc((void*)V, 0, 128u << 20, 0x00020000);
  const unsigned base = ((b * HKV + kvh) * SK + s * 2048 + wave * 32) * 256u;
  const unsigned koff = r * 256 + g * 16, voff = g * 1024 + r * 16;
  u32x4 kb[2][8], vb[2][8];
#define ISSUE(S, T) do { const unsigned o_ = base + (unsigned)(T) * (NW * 8192u); \
    _Pragma("unroll") for (int i = 0; i < 8; ++i) kb[S][i] = __builtin_amdgcn_raw_buffer_load_b128(rk, koff + (i & 3) * 64 + (i >> 2) * 4096, o_, AUXKV); \
    _Pragma("unroll") for (int i = 0; i < 8; ++i) vb[S][i] = __builtin_amdgcn_raw_buffer_load_b128(rv, voff + (i & 3) * 256 + (i >> 2) * 4096, o_, AUXKV); \
    __builtin_amdgcn_sched_barrier(0); } while (0)
  u32x4 qf[4];
  { const float* qp = q + b * 4096 + (kvh * 4 + (r & 3)) * 128 + g * 8;
#pragma unroll
    for (int t = 0; t < 4; ++t) { f32x4 a = *(const f32x4*)(qp + t * 32), c = *(const f32x4*)(qp + t * 32 + 4);
      qf[t] = u32x4{cvtpk(a[0], a[1]), cvtpk(a[2], a[3]), cvtpk(c[0], c[1]), cvtpk(c[2], c[3])}; } }
  __builtin_amdgcn_sched_barrier(0);
  ISSUE(0, 0); ISSUE(1, 1);
  constexpr float C2 = 0.08838834764831845f * 1.4426950408889634f;
  float m = -INFINITY, l = 0.f;
  f32x4 acc[8];
#pragma unroll
  for (int e = 0; e < 8; ++e) acc[e] = f32x4{0, 0, 0, 0};
#define TILE(S) do { \
    f32x4 d0 = {0,0,0,0}, d1 = {0,0,0,0}; \
    _Pragma("unroll") for (int t = 0; t < 4; ++t) { d0 = mfma16(kb[S][t], qf[t], d0); d1 = mfma16(kb[S][4 + t], qf[t], d1); } \
    float tm = fmaxf(fmaxf(fmaxf(d0[0], d0[1]), fmaxf(d0[2], d0[3])), fmaxf(fmaxf(d1[0], d1[1]), fmaxf(d1[2], d1[3]))); \
    tm = xmax(tm); \
    const float mn = fmaxf(m, tm); \
    if (__builtin_amdgcn_ballot_w64(mn != m) != 0) { const float al = __builtin_amdgcn_exp2f((m - mn) * C2); l *= al; \
      _Pragma("unroll") for (int e = 0; e < 8; ++e) acc[e] *= al; m = mn; } \
    const float mc = m * C2; float p[8]; \
    _Pragma("unroll") for (int i = 0; i < 4; ++i) { p[i] = __builtin_amdgcn_exp2f(fmaf(d0[i], C2, -mc)); p[4 + i] = __builtin_amdgcn_exp2f(fmaf(d1[i], C2, -mc)); } \
    l += ((p[0] + p[1]) + (p[2] + p[3])) + ((p[4] + p[5]) + (p[6] + p[7])); \
    const u32x4 pf = {cvtpk(p[0], p[1]), cvtpk(p[2], p[3]), cvtpk(p[4], p[5]), cvtpk(p[6], p[7])}; \
    _Pragma("unroll") for (int w = 0; w < 4; ++w) { \
      u32x4 lo, hi; \
      _Pragma("unroll") for (int jp = 0; jp < 4; ++jp) { lo[jp] = __builtin_amdgcn_perm(vb[S][2 * jp + 1][w], vb[S][2 * jp][w], 0x05040100u); hi[jp] = __builtin_amdgcn_perm(vb[S][2 * jp + 1][w], vb[S][2 * jp][w], 0x07060302u); } \
      acc[2 * w] = mfma16(lo, pf, acc[2 * w]); acc[2 * w + 1] = mfma16(hi, pf, acc[2 * w + 1]); } \
    __builtin_amdgcn_sched_barrier(0); } while (0)
  for (int t = 0; t < NT - 2; t += 2) {
    TILE(0); ISSUE(0, t + 2);
    TILE(1); ISSUE(1, t + 3);
  }
  TILE(0); TILE(1);
  l = xsum(l);
  if (r < 4) {
#pragma unroll
    for (int i = 0; i < 4; ++i) {
      *(f32x4*)&mrg[wave][r][4 + 8 * (4 * g + i)] = f32x4{acc[0][i], acc[1][i], acc[2][i], acc[3][i]};
      *(f32x4*)&mrg[wave][r][8 + 8 * (4 * g + i)] = f32x4{acc[4][i], acc[5][i], acc[6][i], acc[7][i]};
    }
    if (g == 0) { mrg[wave][r][0] = m; mrg[wave][r][1] = l; }
  }
  __syncthreads();
  { const int hh = threadIdx.x >> 7, d = threadIdx.x & 127;
    float M = mrg[0][hh][0];
#pragma unroll
    for (int w = 1; w < NW; ++w) M = fmaxf(M, mrg[w][hh][0]);
    float L = 0, o = 0;
#pragma unroll
    for (int w = 0; w < NW; ++w) { const float wt = __builtin_amdgcn_exp2f((mrg[w][hh][0] - M) * C2); L += wt * mrg[w][hh][1]; o += wt * mrg[w][hh][4 + d]; }
    float* po = part + ((size_t)(b * HQ + kvh * 4 + hh) * NSPLIT + s) * 130;
    po[2 + d] = o; if (d == 0) { po[0] = M * 0.08838834764831845f; po[1] = L; } }
}

__global__ void __launch_bounds__(256) k_post1(const float* part, const u16* wo, const float* h, const u16* w2, float* sp, float* zp) {
  __shared__ float o[2][32], hs[2][14];
  const int t = threadIdx.x, rb = blockIdx.x >> 1, ch = blockIdx.x & 1, col = ch * 256 + t;
  u16 wv[32], w2v[14];
  for (int k = 0; k < 32; ++k) wv[k] = wo[(size_t)(rb * 32 + k) * D + col];
  for (int k = 0; k < 14; ++k) w2v[k] = w2[(size_t)(rb * 14 + k) * D + col];
  if (t < 64) { const int b = t >> 5, f = rb * 32 + (t & 31), hh = f >> 7, d = f & 127;
    const float* p = part + (size_t)(b * HQ + hh) * NSPLIT * 130; float M = -INFINITY; for (int s = 0; s < NSPLIT; ++s) M = fmaxf(M, p[s * 130]);
    float L = 0, a = 0; for (int s = 0; s < NSPLIT; ++s) { float wgt = __expf(p[s * 130] - M); L += wgt * p[s * 130 + 1]; a += wgt * p[s * 130 + 2 + d]; }
    o[b][t & 31] = rbf(a / L); }
  else if (t < 64 + 28) { const int i = t - 64; hs[i / 14][i % 14] = h[(i / 14) * F + rb * 14 + i % 14]; }
  __syncthreads();
  float s0 = 0, s1 = 0, z0 = 0, z1 = 0;
  for (int k = 0; k < 32; ++k) { s0 += o[0][k] * bf2f(wv[k]); s1 += o[1][k] * bf2f(wv[k]); }
  for (int k = 0; k < 14; ++k) { z0 += hs[0][k] * bf2f(w2v[k]); z1 += hs[1][k] * bf2f(w2v[k]); }
  sp[rb * 1024 + col] = s0; sp[rb * 1024 + 512 + col] = s1; zp[rb * 1024 + col] = z0; zp[rb * 1024 + 512 + col] = z1;
}
__global__ void __launch_bounds__(256) k_post2(const u16* x, const float* sp, const float* zp, const u16* b2, u16* out) {
  __shared__ float rs[16][16], rz[16][16];
  const int t = threadIdx.x, c = t & 15, kq = t >> 4, o = blockIdx.x * 16 + c;
  float s = 0, z = 0;
  for (int i = 0; i < 8; ++i) { s += sp[(kq * 8 + i) * 1024 + o]; z += zp[(kq * 8 + i) * 1024 + o]; }
  rs[kq][c] = s; rz[kq][c] = z;
  __syncthreads();
  if (t < 16) { s = 0; z = 0; for (int k = 0; k < 16; ++k) { s += rs[k][c]; z += rz[k][c]; }
    s = rbf(s); float g = rbf(rbf(z) + bf2f(b2[o & 511])); g = rbf(g / (1.f + __expf(-g)));
    out[o] = f2bf(bf2f(x[o]) + rbf(s * g)); }
}

extern "C" void kernel_launch(void* const* d_in, const int* in_sizes, int n_in, void* d_out, int out_size, void* d_ws, size_t ws_size, hipStream_t stream) {
  const u16* x = (const u16*)d_in[0]; const u16* K = (const u16*)d_in[1]; const u16* V = (const u16*)d_in[2];
  const u16* wq = (const u16*)d_in[3]; const u16* wo = (const u16*)d_in[4];
  const u16* w1 = (const u16*)d_in[6]; const u16* b1 = (const u16*)d_in[7]; const u16* w2 = (const u16*)d_in[8]; const u16* b2 = (const u16*)d_in[9];
  float* q = (float*)d_ws; float* h = q + 8192; float* part = h + 4096; float* sp = part + 64 * 16 * 130; float* zp = sp + 128 * 1024;
  k_pre<<<256 + 112, 256, 0, stream>>>(x, wq, w1, b1, q, h);
  k_attn<<<256, NW * 64, 0, stream>>>(K, V, q, part);
  k_post1<<<256, 256, 0, stream>>>(part, wo, h, w2, sp, zp);
  k_post2<<<64, 256, 0, stream>>>(x, sp, zp, b2, (u16*)d_out);
}
```

```cpp
#include <hip/hip_runtime.h>
#include <stdint.h>

typedef unsigned short u16;
typedef unsigned long long u64;
typedef unsigned u32x2 __attribute__((ext_vector_type(2)));
typedef unsigned u32x4 __attribute__((ext_vector_type(4)));
typedef float f32x2 __attribute__((ext_vector_type(2)));
typedef float f32x4 __attribute__((ext_vector_type(4)));
typedef short s16x4 __attribute__((ext_vector_type(4)));
typedef short bf16x8 __attribute__((ext_vector_type(8)));
typedef __bf16 bf16x2_t __attribute__((ext_vector_type(2)));
#define GAS __attribute__((address_space(1)))
#define LAS __attribute__((address_space(3)))
#define CAS __attribute__((address_space(4)))

#define FQ 5
#define PQ 4
#define ROT 0
#define AUXKV 2
#define WQPOL ""
#define QTOUCH 1
#define QDELAY 16
#define TOUCH_AT 12

constexpr int SK = 32768;
constexpr int AUX_SC1 = 16, AUX_NT = 2, RS3 = 0x00020000;
constexpr unsigned WS_G = 0, WS_CF = 4096, WS_QG = 8192, WS_BOX = WS_QG + 16 * 16384;
constexpr unsigned BOX = 4096 + 128, BX_PA = 0, BX_SB = 2048, BX_ZB = 2560, BX_HB = 3072, WS_END = WS_BOX + 256 * BOX;
constexpr int L_WQ = 0, L_WO = 16384, L_W1 = 32768, L_W2 = 40960, L_XN = 49152, L_Y = 51200, L_Q = 53248, L_H = 54272, L_O = 54528,
              L_M = 55040, L_L = 55104, L_PRG = 55168, L_G = 55184, L_A = 55296, A_STR = 528, L_END = L_A + 16 * A_STR;
constexpr unsigned EMPTY = 0xFFFFFFFFu, OOB = 0x80000000u;
constexpr unsigned SPIN_CAP = 1u << 18;
constexpr float LOG2E = 1.4426950408889634f, C2 = 0.08838834764831845f * LOG2E;

#define SCHED() __builtin_amdgcn_sched_barrier(0)
#define STAMP(k) do {} while (0)

__device__ __forceinline__ f32x4 mfma16(u32x4 a, u32x4 b, f32x4 c) {
  return __builtin_amdgcn_mfma_f32_16x16x32_bf16(__builtin_bit_cast(bf16x8, a), __builtin_bit_cast(bf16x8, b), c, 0, 0, 0);
}
__device__ __forceinline__ unsigned cvtpk(float lo, float hi) { return __builtin_bit_cast(unsigned, __builtin_convertvector(f32x2{lo, hi}, bf16x2_t)); }
__device__ __forceinline__ float blo(unsigned u) { return __uint_as_float(u << 16); }
__device__ __forceinline__ float bhi(unsigned u) { return __uint_as_float(u & 0xffff0000u); }
__device__ __forceinline__ float rbf(float x) { return blo(cvtpk(x, x)); }
template <int CTRL> __device__ __forceinline__ float dpp(float x) {
  return __builtin_bit_cast(float, __builtin_amdgcn_mov_dpp(__builtin_bit_cast(int, x), CTRL, 0xf, 0xf, true));
}
constexpr int XOR1 = 0xB1, XOR2 = 0x4E, ROR4 = 0x124, ROR8 = 0x128;
__device__ __forceinline__ float xrow_max(float x) {
  auto s = __builtin_amdgcn_permlane16_swap(__float_as_uint(x), __float_as_uint(x), false, false);
  x = fmaxf(__uint_as_float(s[0]), __uint_as_float(s[1]));
  auto t = __builtin_amdgcn_permlane32_swap(__float_as_uint(x), __float_as_uint(x), false, false);
  return fmaxf(__uint_as_float(t[0]), __uint_as_float(t[1]));
}
__device__ __forceinline__ float xrow_sum(float x) {
  auto s = __builtin_amdgcn_permlane16_swap(__float_as_uint(x), __float_as_uint(x), false, false);
  x = __uint_as_float(s[0]) + __uint_as_float(s[1]);
  auto t = __builtin_amdgcn_permlane32_swap(__float_as_uint(x), __float_as_uint(x), false, false);
  return __uint_as_float(t[0]) + __uint_as_float(t[1]);
}
__device__ __forceinline__ float wave_sum(float x) {
  x += dpp<XOR1>(x); x += dpp<XOR2>(x); x += dpp<ROR4>(x); x += dpp<ROR8>(x); return xrow_sum(x);
}
__device__ __forceinline__ float rows_reduce4(f32x4 v) {
  auto a = __builtin_amdgcn_permlane32_swap(__float_as_uint(v[0]), __float_as_uint(v[2]), false, false);
  auto b = __builtin_amdgcn_permlane32_swap(__float_as_uint(v[1]), __float_as_uint(v[3]), false, false);
  const float u0 = __uint_as_float(a[0]) + __uint_as_float(a[1]), u1 = __uint_as_float(b[0]) + __uint_as_float(b[1]);
  auto c = __builtin_amdgcn_permlane16_swap(__float_as_uint(u0), __float_as_uint(u1), false, false);
  return __uint_as_float(c[0]) + __uint_as_float(c[1]);
}
__device__ __forceinline__ u32x4 ln8(u32x4 v) {
  float f[8];
#pragma unroll
  for (int i = 0; i < 4; ++i) { f[2 * i] = blo(v[i]); f[2 * i + 1] = bhi(v[i]); }
  const float mean = wave_sum(((f[0] + f[1]) + (f[2] + f[3])) + ((f[4] + f[5]) + (f[6] + f[7]))) * (1.f / 512.f);
  float ss = 0.f;
#pragma unroll
  for (int i = 0; i < 8; ++i) { const float d = f[i] - mean; ss = fmaf(d, d, ss); }
  const float var = wave_sum(ss) * (1.f / 512.f);
  const float mb = rbf(mean), inv = __builtin_amdgcn_rcpf(rbf(__builtin_amdgcn_sqrtf(rbf(rbf(var) + 1e-5f))));
  u32x4 o;
#pragma unroll
  for (int i = 0; i < 4; ++i) { const unsigned c = cvtpk(f[2 * i] - mb, f[2 * i + 1] - mb); o[i] = cvtpk(blo(c) * inv, bhi(c) * inv); }
  return o;
}
__device__ __forceinline__ unsigned nz(float x) { return min(__float_as_uint(x), EMPTY - 1u); }
__device__ __forceinline__ u32x4 nz4(f32x4 v) { return u32x4{nz(v[0]), nz(v[1]), nz(v[2]), nz(v[3])}; }
__device__ __forceinline__ bool any_empty(u32x4 v) { return max(max(v[0], v[1]), max(v[2], v[3])) == EMPTY; }
__device__ __forceinline__ u32x4 tr2(LAS unsigned char* p, int o0, int o1) {
  const u32x2 a = __builtin_bit_cast(u32x2, __builtin_amdgcn_ds_read_tr16_b64_v4i16((LAS s16x4*)(p + o0)));
  const u32x2 b = __builtin_bit_cast(u32x2, __builtin_amdgcn_ds_read_tr16_b64_v4i16((LAS s16x4*)(p + o1)));
  return u32x4{a[0], a[1], b[0], b[1]};
}
__device__ __forceinline__ f32x4 gemv64(LAS unsigned char* img, LAS unsigned char* bp, int g, int r) {
  LAS unsigned char* const a = img + (8 * g + (r >> 2)) * 128 + (r & 3) * 8;
  const u32x4 b0 = *(LAS u32x4*)(bp + g * 16), b1 = *(LAS u32x4*)(bp + 64 + g * 16);
  f32x4 z[4];
#pragma unroll
  for (int n = 0; n < 4; ++n) {
    z[n] = mfma16(tr2(a, n * 32, n * 32 + 512), b0, f32x4{0.f, 0.f, 0.f, 0.f});
    z[n] = mfma16(tr2(a, 4096 + n * 32, 4096 + n * 32 + 512), b1, z[n]);
  }
  return (r & 4) ? ((r & 2) ? z[3] : z[2]) : ((r & 2) ? z[1] : z[0]);
}
#define BAR() asm volatile("s_waitcnt lgkmcnt(0)\n\ts_barrier" ::: "memory")
#define VM0() __builtin_amdgcn_s_waitcnt(0x0F70)
#define FENCE() asm volatile("" ::: "memory")
#define VMW(N) do { __builtin_amdgcn_s_waitcnt(0x0F70 | ((N) & 15) | (((N) >> 4) << 14)); SCHED(); } while (0)

__global__ void __launch_bounds__(512, 2)
fused_decode(const u16* a_x, const u16* a_k, const u16* a_v, const u16* a_wq, const u16* a_wo, const u16* a_w1, const u16* a_b1, const u16* a_w2, const u16* a_b2,
             u16* a_out, unsigned char* a_ws) {
  __shared__ __attribute__((aligned(16))) unsigned char smem[L_END];
  LAS unsigned char* const L = (LAS unsigned char*)smem;
#define ARGS() const CAS u64* kp_ = (const CAS u64*)__builtin_amdgcn_kernarg_segment_ptr(); asm volatile("" : "+s"(kp_)); \
  const u16* const x = (const u16*)kp_[0]; const u16* const Kc = (const u16*)kp_[1]; const u16* const Vc = (const u16*)kp_[2]; const u16* const wq = (const u16*)kp_[3]; \
  const u16* const wo = (const u16*)kp_[4]; const u16* const w1 = (const u16*)kp_[5]; const u16* const b1 = (const u16*)kp_[6]; const u16* const w2 = (const u16*)kp_[7]; \
  const u16* const b2 = (const u16*)kp_[8]; u16* const out = (u16*)kp_[9]; unsigned char* const ws = (unsigned char*)kp_[10]; \
  const auto rw = __builtin_amdgcn_make_buffer_rsrc(ws, 0, WS_END, RS3); \
  (void)x; (void)Kc; (void)Vc; (void)wq; (void)wo; (void)w1; (void)b1; (void)w2; (void)b2; (void)out; (void)rw
  const int lane = threadIdx.x & 63;
  int wave = __builtin_amdgcn_readfirstlane(threadIdx.x >> 6);
  const int xcd = blockIdx.x & 7, j = blockIdx.x >> 3, lw = xcd * 32 + j;
  const int g = lane >> 4, r = lane & 15;
  const unsigned box = WS_BOX + lw * BOX;
  float gate = 0.f, xres = 0.f;
  u64 gnext = 0;

  if (wave < 4) {
    ARGS();
    asm volatile("" :: "s"(ws));
    u32x4 xr;
    __builtin_amdgcn_s_setprio(3);
    {
      const u64 qb = (u64)(wq + (j & 3) * 128 * 4096 + xcd * 512 + (j >> 2) * 64);
      const u32x4 rq = {(unsigned)qb, (unsigned)(qb >> 32) & 0xffffu, 127u * 8192u + 128u, (unsigned)RS3};
      const unsigned vo = (wave * 32 + (lane >> 3)) * 8192 + (lane & 7) * 16;
      asm volatile("s_mov_b32 m0, %3\n\ts_nop 3\n\tglobal_load_dwordx4 %0, %7, %8\n\t"
                   "buffer_load_dwordx4 %1, %2, 0 offen nt lds\n\t"
                   "s_add_u32 m0, m0, 0x400\n\ts_nop 0\n\tbuffer_load_dwordx4 %1, %2, %4 offen nt lds\n\t"
                   "s_add_u32 m0, m0, 0x400\n\ts_nop 0\n\tbuffer_load_dwordx4 %1, %2, %5 offen nt lds\n\t"
                   "s_add_u32 m0, m0, 0x400\n\ts_nop 0\n\tbuffer_load_dwordx4 %1, %2, %6 offen nt lds"
                   : "=&v"(xr)
                   : "v"(vo), "s"(rq), "s"((unsigned)(size_t)(L + L_WQ) + wave * 4096), "s"(0x10000u), "s"(0x20000u), "s"(0x30000u),
                     "v"((wave & 1) * 1024 + lane * 16), "s"(x) : "memory", "scc");
    }
    __builtin_amdgcn_s_setprio(0);
    if (wave == 0) STAMP(21);
    const size_t kvoff = ((size_t)((j & 1) * 8 + xcd) * SK + (j >> 1) * 2048) * 128;
    const auto rk = __builtin_amdgcn_make_buffer_rsrc((void*)(Kc + kvoff), 0, 2048 * 256, RS3);
    const auto rv = __builtin_amdgcn_make_buffer_rsrc((void*)(Vc + kvoff), 0, 2048 * 256, RS3);
    const unsigned koff = r * 256 + g * 16, voff = g * 1024 + r * 16, wb = wave * 8192u;
    u32x4 kb[2][8], vb[2][8];
#define ISSUE(H, T) do { const unsigned o_ = wb + (unsigned)((T) + ((H) >> 3)) * 32768u + (((H) >> 1) & 1) * 4096u; \
    if (((H) & 4) == 0) { _Pragma("unroll") for (int i = 0; i < 2; ++i) kb[((H) >> 3) & 1][((H) & 3) * 2 + i] = __builtin_amdgcn_raw_buffer_load_b128(rk, koff + (((H) & 1) * 2 + i) * 64, o_, AUXKV); } \
    else { _Pragma("unroll") for (int i = 0; i < 2; ++i) vb[((H) >> 3) & 1][((H) & 3) * 2 + i] = __builtin_amdgcn_raw_buffer_load_b128(rv, voff + (((H) & 1) * 2 + i) * 256, o_, AUXKV); } \
    SCHED(); } while (0)
#define PRE(H) do { if ((H) == PQ) { \
      if (wave == 0) STAMP(24); \
      asm volatile("s_waitcnt vmcnt(%1)" : "+v"(xr) : "n"(4 + 2 * PQ)); \
      if (wave == 0) STAMP(25); \
      *(LAS u32x4*)(L + L_XN + (wave & 1) * 1024 + lane * 16) = ln8(xr); \
      if (wave == 0) STAMP(16); \
      asm volatile("s_waitcnt vmcnt(%0) lgkmcnt(0)\n\ts_barrier" :: "n"(2 * PQ) : "memory");   } \
    if ((H) >= PQ) VMW(2 * (PQ - 1)); ISSUE(H, 0); } while (0)
    SCHED();
    PRE(0); PRE(1); PRE(2); PRE(3); PRE(4); PRE(5); PRE(6); PRE(7);
    if (wave == 0) STAMP(22);
    PRE(8); PRE(9); PRE(10); PRE(11); PRE(12); PRE(13); PRE(14); PRE(15);
    if (wave == 0) STAMP(1);
    BAR();
    if (wave == 0) STAMP(2);
    u32x4 qf[4];
#pragma unroll
    for (int t = 0; t < 4; ++t) qf[t] = *(LAS u32x4*)(L + L_Q + (r & 3) * 256 + t * 64 + g * 16);
    float m = -INFINITY, l = 0.f;
    f32x4 acc[8];
#pragma unroll
    for (int e = 0; e < 8; ++e) acc[e] = f32x4{0.f, 0.f, 0.f, 0.f};
    u32x4 pf;
#define QK(S) do { \
    f32x4 d0 = {0.f, 0.f, 0.f, 0.f}, d1 = {0.f, 0.f, 0.f, 0.f}; \
    _Pragma("unroll") for (int t = 0; t < 4; ++t) { d0 = mfma16(kb[S][t], qf[t], d0); d1 = mfma16(kb[S][4 + t], qf[t], d1); } \
    const float mn = fmaxf(m, xrow_max(fmaxf(fmaxf(fmaxf(d0[0], d0[1]), fmaxf(d0[2], d0[3])), fmaxf(fmaxf(d1[0], d1[1]), fmaxf(d1[2], d1[3]))))); \
    if (__builtin_amdgcn_ballot_w64(mn != m) != 0) { const float al = __builtin_amdgcn_exp2f((m - mn) * C2); l *= al; \
      _Pragma("unroll") for (int e = 0; e < 8; ++e) acc[e] *= al; m = mn; } \
    const float mc = m * C2; float p[8]; \
    _Pragma("unroll") for (int i = 0; i < 4; ++i) { p[i] = __builtin_amdgcn_exp2f(fmaf(d0[i], C2, -mc)); p[4 + i] = __builtin_amdgcn_exp2f(fmaf(d1[i], C2, -mc)); } \
    l += ((p[0] + p[1]) + (p[2] + p[3])) + ((p[4] + p[5]) + (p[6] + p[7])); \
    pf = u32x4{cvtpk(p[0], p[1]), cvtpk(p[2], p[3]), cvtpk(p[4], p[5]), cvtpk(p[6], p[7])}; SCHED(); } while (0)
#define PV(S) do { \
    _Pragma("unroll") for (int w = 0; w < 4; ++w) { u32x4 lo, hi; \
      _Pragma("unroll") for (int jp = 0; jp < 4; ++jp) { lo[jp] = __builtin_amdgcn_perm(vb[S][2 * jp + 1][w], vb[S][2 * jp][w], 0x05040100u); hi[jp] = __builtin_amdgcn_perm(vb[S][2 * jp + 1][w], vb[S][2 * jp][w], 0x07060302u); } \
      acc[2 * w] = mfma16(lo, pf, acc[2 * w]); acc[2 * w + 1] = mfma16(hi, pf, acc[2 * w + 1]); } SCHED(); } while (0)
#define CATCH(H) do { if ((H) < FQ) ISSUE(16 + (H), 0); } while (0)
    QK(0); CATCH(0); CATCH(1); CATCH(2); CATCH(3);
    PV(0); CATCH(4); CATCH(5); CATCH(6); CATCH(7);
    QK(1); CATCH(8); CATCH(9); CATCH(10); CATCH(11);
    PV(1);
    if (wave == 0) STAMP(3);
#define STEP(H) do { VMW(2 * (FQ - 1)); ISSUE((H) + FQ, t); } while (0)
#define PACE(H) do {} while (0)
#define STEP4(H) do { PACE(H); STEP(H); STEP((H) + 1); STEP((H) + 2); STEP((H) + 3); } while (0)
#pragma unroll 1
    for (int t = 2; t < 16; t += 2) {
      *(volatile LAS int*)(L + L_PRG) = t;
      STAMP(32 + wave * 8 + (t >> 1) - 1);
      if (ROT) { const int pr = (wave + (t >> 1)) & 3; if (pr == 0) __builtin_amdgcn_s_setprio(2); else if (pr == 1) __builtin_amdgcn_s_setprio(1); else __builtin_amdgcn_s_setprio(0); }
      STEP4(0); QK(0);
      STEP4(4); PV(0);
      STEP4(8); QK(1);
      STEP4(12); PV(1);
    }
    if (wave == 0) STAMP(4);
    STAMP(32 + wave * 8 + 7);
    l = xrow_sum(l);
    if (r < 4) {
#pragma unroll
      for (int e = 0; e < 8; ++e) *(LAS f32x4*)(L + L_A + (wave * 4 + r) * A_STR + e * 64 + g * 16) = acc[e];
      *(LAS float*)(L + L_M + r * 16 + wave * 4) = m; *(LAS float*)(L + L_L + r * 16 + wave * 4) = l;
    }
  }
  asm volatile("" : "+s"(wave));
  if (wave >= 4) {
    __builtin_amdgcn_s_setprio(2);
    ARGS();
    gnext = *(const CAS u64*)(ws + WS_G) + 1;
    if (wave == 4) STAMP(15);
    __builtin_amdgcn_raw_buffer_store_b128(u32x4{EMPTY, EMPTY, EMPTY, EMPTY}, rw, box + (wave - 4) * 1024 + lane * 16, 0, AUX_SC1);
    const unsigned qsrc = WS_QG + (xcd * 2 + (j & 1)) * 16384 + (wave - 4) * 1024 + lane * 16;
    const unsigned t0 = __builtin_amdgcn_raw_buffer_load_b32(rw, (QTOUCH && lane < 2) ? WS_QG + (xcd * 2 + (j & 1)) * 16384 + (j >> 1) * 1024 + (wave - 4) * 256 + lane * 128 : OOB, 0, AUX_SC1);
    u32x4 v[4];
    *(volatile LAS int*)(L + L_PRG) = 0;
    BAR();
    if (wave == 4) STAMP(8);
    LAS unsigned char* const ap = L + L_WQ + (8 * g + (r >> 2)) * 128 + (r & 3) * 8 + (wave - 4) * 32;
    LAS unsigned char* const bp = L + L_XN + (r & 1) * 1024 + (j & 3) * 256 + g * 16;
    f32x4 qv = {0.f, 0.f, 0.f, 0.f};
#pragma unroll
    for (int k = 0; k < 4; ++k) qv = mfma16(tr2(ap, k * 4096, k * 4096 + 512), *(LAS u32x4*)(bp + k * 64), qv);
    const unsigned tl = (unsigned)gnext & 0xffu, th = (unsigned)(gnext >> 8);
    const unsigned q0 = __float_as_uint((r & 2) ? qv[2] : qv[0]) + 0x80u, q1 = __float_as_uint((r & 2) ? qv[3] : qv[1]) + 0x80u;
    __builtin_amdgcn_raw_buffer_store_b128(u32x4{(q0 & 0xffffff00u) | tl, th, (q1 & 0xffffff00u) | tl, th}, rw,
                                           r < 4 ? WS_QG + (xcd * 2 + (r & 1)) * 16384 + (j & 3) * 4096 + ((j >> 2) * 64 + (wave - 4) * 16 + g * 4 + (r & 2)) * 8 : OOB, 0, AUX_SC1);
    if (wave == 4) STAMP(9);
    __builtin_amdgcn_s_setprio(3);
    asm volatile("" :: "v"(t0));
    if (QDELAY) __builtin_amdgcn_s_sleep(QDELAY);
#pragma unroll 1
    for (unsigned spin = 0; spin < SPIN_CAP; ++spin) {
      FENCE();
      unsigned bad = 0;
#pragma unroll
      for (int k = 0; k < 4; ++k) v[k] = __builtin_amdgcn_raw_buffer_load_b128(rw, qsrc + k * 4096, 0, AUX_SC1);
#pragma unroll
      for (int k = 0; k < 4; ++k) bad |= ((v[k][0] ^ tl) << 24) | (v[k][1] ^ th) | ((v[k][2] ^ tl) << 24) | (v[k][3] ^ th);
      if (__builtin_amdgcn_ballot_w64(bad != 0) == 0) break;
    }
    float s0 = 0.f, s1 = 0.f;
#pragma unroll
    for (int k = 0; k < 4; ++k) { s0 += __uint_as_float(v[k][0] & 0xffffff00u); s1 += __uint_as_float(v[k][2] & 0xffffff00u); }
    *(LAS unsigned*)(L + L_Q + (wave - 4) * 256 + lane * 4) = cvtpk(s0, s1);
    if (wave == 4) STAMP(10);
    asm volatile("s_waitcnt vmcnt(0) lgkmcnt(0)\n\ts_barrier" ::: "memory");
  }
  asm volatile("" : "+s"(wave));
  if (wave == 5) {
    ARGS();
    const unsigned tl = (unsigned)gnext, th = (unsigned)(gnext >> 32);
    __builtin_amdgcn_raw_buffer_store_b64(u32x2{tl, th}, rw, lane == 0 ? WS_CF + lw * 8 : OOB, 0, AUX_SC1);
    const bool mlp = lw < 224;
    const int team = mlp ? lw >> 3 : 0, mu = lw & 7;
    u32x4 x0 = *(const GAS u32x4*)(x + lane * 8), x1 = *(const GAS u32x4*)(x + 512 + lane * 8);
    const int hcol = (lane >> 4) * 16 + (lane & 7) * 2;
    const unsigned bb = *(const GAS unsigned*)(b1 + team * 64 + hcol);
    const int ocol = xcd * 64 + (j & 15) * 4 + g;
    xres = blo(((const GAS u16*)x)[(lane & 1) * 512 + ocol]);
    const float bias = blo(((const GAS u16*)b2)[ocol]);
    const auto r1 = __builtin_amdgcn_make_buffer_rsrc((void*)(w1 + mu * 64 * 1792 + team * 64), 0, mlp ? 63 * 3584 + 128 : 0, RS3);
    const auto r2 = __builtin_amdgcn_make_buffer_rsrc((void*)(w2 + team * 64 * 512 + mu * 64), 0, mlp ? 63 * 1024 + 128 : 0, RS3);
    const auto ro = __builtin_amdgcn_make_buffer_rsrc((void*)(wo + (xcd * 512 + (j >> 3) * 128) * 512 + (j & 7) * 64), 0, 127 * 1024 + 128, RS3);
#pragma unroll
    for (int i = 0; i < 8; ++i) __builtin_amdgcn_raw_ptr_buffer_load_lds(r1, L + L_W1 + i * 1024, 16, (lane >> 3) * 3584 + (lane & 7) * 16, i * 8 * 3584, 0, AUX_NT);
    SCHED();
    asm volatile("" : "+v"(x0), "+v"(x1));
    *(LAS u32x4*)(L + L_Y + lane * 16) = ln8(ln8(x0));
    *(LAS u32x4*)(L + L_Y + 1024 + lane * 16) = ln8(ln8(x1));
    VM0();
    STAMP(17);
#pragma unroll
    for (int i = 0; i < 8; ++i) __builtin_amdgcn_raw_ptr_buffer_load_lds(r2, L + L_W2 + i * 1024, 16, (lane >> 3) * 1024 + (lane & 7) * 16, i * 8 * 1024, 0, AUX_NT);
#pragma unroll 1
    for (unsigned spin = 0; spin < SPIN_CAP; ++spin) {
      FENCE();
      const u32x4 a = __builtin_amdgcn_raw_buffer_load_b128(rw, WS_CF + lane * 16, 0, AUX_SC1), c = __builtin_amdgcn_raw_buffer_load_b128(rw, WS_CF + 1024 + lane * 16, 0, AUX_SC1);
      const unsigned bad = (a[0] ^ tl) | (a[1] ^ th) | (a[2] ^ tl) | (a[3] ^ th) | (c[0] ^ tl) | (c[1] ^ th) | (c[2] ^ tl) | (c[3] ^ th);
      if (__builtin_amdgcn_ballot_w64(bad != 0) == 0) break;
      __builtin_amdgcn_s_sleep(8);
    }
    VM0();
    STAMP(20);
    const unsigned o64 = r < 8 ? (r >> 1) * 128 + (r & 1) * 64 + g * 16 : OOB;
    if (mlp) {
      __builtin_amdgcn_raw_buffer_store_b128(nz4(gemv64(L + L_W1, L + L_Y + (r & 1) * 1024 + mu * 128, g, r)), rw, box + BX_HB + o64, 0, AUX_SC1);
      STAMP(18);
    }
#pragma unroll
    for (int i = 0; i < 16; ++i) __builtin_amdgcn_raw_ptr_buffer_load_lds(ro, L + L_WO + i * 1024, 16, (lane >> 3) * 8192 + (lane & 7) * 16, (i & 1) * 65536 + (i >> 1) * 1024, 0, AUX_NT);
    if (mlp) {
      u32x2 v[8];
#pragma unroll 1
      for (unsigned spin = 0; spin < SPIN_CAP; ++spin) {
        FENCE();
        unsigned mx = 0;
#pragma unroll
        for (int k = 0; k < 8; ++k) v[k] = __builtin_amdgcn_raw_buffer_load_b64(rw, WS_BOX + (team * 8 + k) * BOX + BX_HB + lane * 8, 0, AUX_SC1);
#pragma unroll
        for (int k = 0; k < 8; ++k) mx = max(mx, max(v[k][0], v[k][1]));
        if (__builtin_amdgcn_ballot_w64(mx == EMPTY) == 0) break;
        __builtin_amdgcn_s_sleep(8);
      }
      STAMP(19);
      float hh[2];
#pragma unroll
      for (int i = 0; i < 2; ++i) {
        float s = 0.f;
#pragma unroll
        for (int k = 0; k < 8; ++k) s += __uint_as_float(v[k][i]);
        const float z = rbf(rbf(s) + (i ? bhi(bb) : blo(bb)));
        const float u = 0.7978845608028654f * (z + 0.044715f * z * z * z);
        hh[i] = z * __builtin_amdgcn_rcpf(1.f + __builtin_amdgcn_exp2f(-2.f * LOG2E * u));
      }
      *(LAS unsigned*)(L + L_H + ((lane >> 3) & 1) * 128 + hcol * 2) = cvtpk(hh[0], hh[1]);
      __builtin_amdgcn_raw_buffer_store_b128(nz4(gemv64(L + L_W2, L + L_H + (r & 1) * 128, g, r)), rw, box + BX_ZB + o64, 0, AUX_SC1);
      STAMP(11);
    }
    if (j < 16) {
      const unsigned src = lane < 56 ? WS_BOX + ((lane >> 1) * 8 + xcd) * BOX + BX_ZB + (j >> 2) * 128 + (lane & 1) * 64 + (j & 3) * 16 : OOB;
      u32x4 v;
#pragma unroll 1
      for (unsigned spin = 0; spin < SPIN_CAP; ++spin) {
        FENCE();
        v = __builtin_amdgcn_raw_buffer_load_b128(rw, src, 0, AUX_SC1);
        if (__builtin_amdgcn_ballot_w64(any_empty(v)) == 0) break;
        __builtin_amdgcn_s_sleep(8);
      }
      float z = rows_reduce4(__builtin_bit_cast(f32x4, v));
      z += dpp<XOR2>(z); z += dpp<ROR4>(z); z += dpp<ROR8>(z);
      z = rbf(rbf(z) + bias);
      gate = rbf(z * __builtin_amdgcn_rcpf(1.f + __builtin_amdgcn_exp2f(-LOG2E * z)));
      STAMP(12);
    }
    VM0();
  }
  asm volatile("" : "+s"(wave));
  ARGS();
  const unsigned psrc = WS_BOX + (xcd * 32 + (lane >> 2) * 2 + (wave >> 2)) * BOX + BX_PA + (j >> 3) * 512 + (wave & 3) * 128 + (lane & 3) * 16;
  const unsigned ssrc = WS_BOX + ((lane >> 3) * 32 + ((lane >> 1) & 3) * 8 + xcd) * BOX + BX_SB + ((j >> 2) & 3) * 128 + (lane & 1) * 64 + (j & 3) * 16;
  if (wave >= 6) {
    if (TOUCH_AT >= 0) {
#pragma unroll 1
      for (unsigned spin = 0; spin < SPIN_CAP; ++spin) { if (*(volatile LAS int*)(L + L_PRG) >= TOUCH_AT) break; __builtin_amdgcn_s_sleep(32); }
      const unsigned b0 = WS_BOX + (xcd * 32 + (lane >> 2) * 2 + (wave & 1)) * BOX + BX_PA + (j >> 3) * 512 + (lane & 3) * 128;
      const unsigned t0 = __builtin_amdgcn_raw_buffer_load_b32(rw, b0, 0, AUX_SC1), t1 = __builtin_amdgcn_raw_buffer_load_b32(rw, ssrc, 0, AUX_SC1);
      asm volatile("" :: "v"(t0), "v"(t1));
    }
  }

  BAR();
  if (wave == 0) STAMP(5);
  if (wave == 4 || wave == 5) {
    const int t = (wave - 4) * 64 + lane, hh = t >> 5, d4 = t & 31;
    const f32x4 mm = *(LAS f32x4*)(L + L_M + hh * 16), ll = *(LAS f32x4*)(L + L_L + hh * 16);
    f32x4 a[4];
#pragma unroll
    for (int w = 0; w < 4; ++w) a[w] = *(LAS f32x4*)(L + L_A + (w * 4 + hh) * A_STR + d4 * 16);
    const float M = fmaxf(fmaxf(mm[0], mm[1]), fmaxf(mm[2], mm[3]));
    float wt[4];
#pragma unroll
    for (int w = 0; w < 4; ++w) wt[w] = __builtin_amdgcn_exp2f((mm[w] - M) * C2);
    const float Ls = (ll[0] * wt[0] + ll[1] * wt[1]) + (ll[2] * wt[2] + ll[3] * wt[3]);
    const f32x4 o = ((a[0] * wt[0] + a[1] * wt[1]) + (a[2] * wt[2] + a[3] * wt[3])) * __builtin_amdgcn_rcpf(Ls);
    const unsigned lse = __float_as_uint(fmaf(M, C2, __builtin_amdgcn_logf(Ls)));
    u32x4 e;
#pragma unroll
    for (int i = 0; i < 4; ++i) e[i] = __builtin_amdgcn_perm(min(__float_as_uint(o[i]), 0xFFFFFE00u) + 0x80u, lse, 0x07060500u + i);
    __builtin_amdgcn_raw_buffer_store_b128(e, rw, box + BX_PA + t * 16, 0, AUX_SC1);
    if (wave == 4) STAMP(13);
  }
  LAS unsigned char* const aw = L + L_WO + (8 * g + (r >> 2)) * 128 + (r & 3) * 8 + (wave & 3) * 32;
  u32x4 wa[4];
#pragma unroll
  for (int k = 0; k < 4; ++k) wa[k] = tr2(aw, k * 4096, k * 4096 + 512);
  {
    u32x4 v0, v1;
#pragma unroll 1
    for (unsigned spin = 0; spin < SPIN_CAP; ++spin) {
      FENCE();
      v0 = __builtin_amdgcn_raw_buffer_load_b128(rw, psrc, 0, AUX_SC1);
      v1 = __builtin_amdgcn_raw_buffer_load_b128(rw, psrc + 64, 0, AUX_SC1);
      if (__builtin_amdgcn_ballot_w64(any_empty(v0) || any_empty(v1)) == 0) break;
    }
    if (wave == 0) STAMP(6);
    const float lse = __uint_as_float(__builtin_amdgcn_perm(__builtin_amdgcn_perm(v0[3], v0[2], 0x04000000u), __builtin_amdgcn_perm(v0[1], v0[0], 0x00000400u), 0x07060100u));
    float M = fmaxf(lse, dpp<ROR4>(lse)); M = fmaxf(M, dpp<ROR8>(M)); M = xrow_max(M);
    const float wt = __builtin_amdgcn_exp2f(lse - M);
    float Ws = wt + dpp<ROR4>(wt); Ws += dpp<ROR8>(Ws); Ws = xrow_sum(Ws);
    float o0 = rows_reduce4(__builtin_bit_cast(f32x4, v0 & 0xFFFFFF00u) * wt), o1 = rows_reduce4(__builtin_bit_cast(f32x4, v1 & 0xFFFFFF00u) * wt);
    o0 += dpp<ROR4>(o0); o1 += dpp<ROR4>(o1); o0 += dpp<ROR8>(o0); o1 += dpp<ROR8>(o1);
    const float inv = __builtin_amdgcn_rcpf(Ws);
    const float o = ((lane & 4) ? o1 : o0) * inv;
    *(LAS u16*)(L + L_O + (wave >> 2) * 256 + ((wave & 3) * 32 + (lane & 4) * 4 + (lane & 3) * 4 + g) * 2) = (u16)cvtpk(o, o);
  }
  BAR();
  if (wave < 4) {
    f32x4 s = {0.f, 0.f, 0.f, 0.f};
#pragma unroll
    for (int k = 0; k < 4; ++k) s = mfma16(wa[k], *(LAS u32x4*)(L + L_O + (r & 1) * 256 + k * 64 + g * 16), s);
    __builtin_amdgcn_raw_buffer_store_b128(nz4(s), rw, r < 2 ? box + BX_SB + wave * 128 + r * 64 + g * 16 : OOB, 0, AUX_SC1);
    if (wave == 0) STAMP(14);
  }
  if (wave == 5 && j < 16) {
    u32x4 v;
#pragma unroll 1
    for (unsigned spin = 0; spin < SPIN_CAP; ++spin) {
      FENCE();
      v = __builtin_amdgcn_raw_buffer_load_b128(rw, ssrc, 0, AUX_SC1);
      if (__builtin_amdgcn_ballot_w64(any_empty(v)) == 0) break;
    }
    float s = rows_reduce4(__builtin_bit_cast(f32x4, v));
    s += dpp<XOR2>(s); s += dpp<ROR4>(s); s += dpp<ROR8>(s);
    const float res = xres + rbf(rbf(s) * gate);
    const auto rout = __builtin_amdgcn_make_buffer_rsrc(out, 0, 2048, RS3);
    __builtin_amdgcn_raw_buffer_store_b16((u16)cvtpk(res, res), rout, r < 2 ? (r * 512 + xcd * 64 + j * 4 + g) * 2 : OOB, 0, 0);
    __builtin_amdgcn_raw_buffer_store_b64(u32x2{(unsigned)gnext, (unsigned)(gnext >> 32)}, rw, (blockIdx.x == 0 && lane == 0) ? WS_G : OOB, 0, AUX_SC1);
    STAMP(7);
  }
}

extern "C" void kernel_launch(void* const* d_in, const int* in_sizes, int n_in, void* d_out, int out_size, void* d_ws, size_t ws_size, hipStream_t stream) {
  fused_decode<<<256, 512, 0, stream>>>((const u16*)d_in[0], (const u16*)d_in[1], (const u16*)d_in[2], (const u16*)d_in[3], (const u16*)d_in[4],
                                        (const u16*)d_in[6], (const u16*)d_in[7], (const u16*)d_in[8], (const u16*)d_in[9], (u16*)d_out, (unsigned char*)d_ws);
}
```

```cpp
#include <hip/hip_runtime.h>
#include <stdint.h>

typedef unsigned short u16;
typedef unsigned long long u64;
typedef unsigned u32x2 __attribute__((ext_vector_type(2)));
typedef unsigned u32x4 __attribute__((ext_vector_type(4)));
typedef float f32x2 __attribute__((ext_vector_type(2)));
typedef float f32x4 __attribute__((ext_vector_type(4)));
typedef short s16x4 __attribute__((ext_vector_type(4)));
typedef short bf16x8 __attribute__((ext_vector_type(8)));
typedef __bf16 bf16x2_t __attribute__((ext_vector_type(2)));
#define GAS __attribute__((address_space(1)))
#define LAS __attribute__((address_space(3)))
#define CAS __attribute__((address_space(4)))

#define FQ 8
#define PQ 5
#define AUXKV 2
#define AUXK AUXKV
#define AUXV AUXKV
#define ROT 0
#define AT1 2
#define AT2 6
#define AT3 10
#define LK 12
#define NUMA 0
#define TOUCH_AT -1

constexpr int SK = 32768;
constexpr int AUX_SC1 = 16, AUX_NT = 2, RS3 = 0x00020000;
constexpr unsigned WS_G = 0, WS_CF = 4096, WS_QG = 8192, WS_BOX = WS_QG + 16 * 16384;
constexpr unsigned BOX = 4096 + 128, BX_PA = 0, BX_SB = 2048, BX_ZB = 2560, BX_HB = 3072, WS_END = WS_BOX + 256 * BOX;
constexpr int L_WQ = 0, L_WO = 16384, L_W1 = 32768, L_W2 = 40960, L_XN = 49152, L_Y = 51200, L_Q = 53248, L_H = 54272, L_O = 54528,
              L_M = 55040, L_L = 55104, L_PRG = 55168, L_A = 55296, A_STR = 528, L_END = L_A + 16 * A_STR;
constexpr unsigned EMPTY = 0xFFFFFFFFu, OOB = 0x80000000u;
constexpr unsigned SPIN_CAP = 1u << 18;
constexpr float LOG2E = 1.4426950408889634f, C2 = 0.08838834764831845f * LOG2E;

#define SCHED() __builtin_amdgcn_sched_barrier(0)
#define STAMP(k) do {} while (0)

__device__ __forceinline__ f32x4 mfma16(u32x4 a, u32x4 b, f32x4 c) {
  return __builtin_amdgcn_mfma_f32_16x16x32_bf16(__builtin_bit_cast(bf16x8, a), __builtin_bit_cast(bf16x8, b), c, 0, 0, 0);
}
__device__ __forceinline__ unsigned cvtpk(float lo, float hi) { return __builtin_bit_cast(unsigned, __builtin_convertvector(f32x2{lo, hi}, bf16x2_t)); }
__device__ __forceinline__ float blo(unsigned u) { return __uint_as_float(u << 16); }
__device__ __forceinline__ float bhi(unsigned u) { return __uint_as_float(u & 0xffff0000u); }
__device__ __forceinline__ float rbf(float x) { return blo(cvtpk(x, x)); }
template <int CTRL> __device__ __forceinline__ float dpp(float x) {
  return __builtin_bit_cast(float, __builtin_amdgcn_mov_dpp(__builtin_bit_cast(int, x), CTRL, 0xf, 0xf, true));
}
constexpr int XOR1 = 0xB1, XOR2 = 0x4E, ROR4 = 0x124, ROR8 = 0x128;
__device__ __forceinline__ float xrow_max(float x) {
  auto s = __builtin_amdgcn_permlane16_swap(__float_as_uint(x), __float_as_uint(x), false, false);
  x = fmaxf(__uint_as_float(s[0]), __uint_as_float(s[1]));
  auto t = __builtin_amdgcn_permlane32_swap(__float_as_uint(x), __float_as_uint(x), false, false);
  return fmaxf(__uint_as_float(t[0]), __uint_as_float(t[1]));
}
__device__ __forceinline__ float xrow_sum(float x) {
  auto s = __builtin_amdgcn_permlane16_swap(__float_as_uint(x), __float_as_uint(x), false, false);
  x = __uint_as_float(s[0]) + __uint_as_float(s[1]);
  auto t = __builtin_amdgcn_permlane32_swap(__float_as_uint(x), __float_as_uint(x), false, false);
  return __uint_as_float(t[0]) + __uint_as_float(t[1]);
}
__device__ __forceinline__ float wave_sum(float x) {
  x += dpp<XOR1>(x); x += dpp<XOR2>(x); x += dpp<ROR4>(x); x += dpp<ROR8>(x); return xrow_sum(x);
}
__device__ __forceinline__ float rows_reduce4(f32x4 v) {
  auto a = __builtin_amdgcn_permlane32_swap(__float_as_uint(v[0]), __float_as_uint(v[2]), false, false);
  auto b = __builtin_amdgcn_permlane32_swap(__float_as_uint(v[1]), __float_as_uint(v[3]), false, false);
  const float u0 = __uint_as_float(a[0]) + __uint_as_float(a[1]), u1 = __uint_as_float(b[0]) + __uint_as_float(b[1]);
  auto c = __builtin_amdgcn_permlane16_swap(__float_as_uint(u0), __float_as_uint(u1), false, false);
  return __uint_as_float(c[0]) + __uint_as_float(c[1]);
}
__device__ __forceinline__ u32x4 ln8(u32x4 v) {
  float f[8];
#pragma unroll
  for (int i = 0; i < 4; ++i) { f[2 * i] = blo(v[i]); f[2 * i + 1] = bhi(v[i]); }
  const float mean = wave_sum(((f[0] + f[1]) + (f[2] + f[3])) + ((f[4] + f[5]) + (f[6] + f[7]))) * (1.f / 512.f);
  float ss = 0.f;
#pragma unroll
  for (int i = 0; i < 8; ++i) { const float d = f[i] - mean; ss = fmaf(d, d, ss); }
  const float var = wave_sum(ss) * (1.f / 512.f);
  const float mb = rbf(mean), inv = __builtin_amdgcn_rcpf(rbf(__builtin_amdgcn_sqrtf(rbf(rbf(var) + 1e-5f))));
  u32x4 o;
#pragma unroll
  for (int i = 0; i < 4; ++i) { const unsigned c = cvtpk(f[2 * i] - mb, f[2 * i + 1] - mb); o[i] = cvtpk(blo(c) * inv, bhi(c) * inv); }
  return o;
}
__device__ __forceinline__ unsigned nz(float x) { return min(__float_as_uint(x), EMPTY - 1u); }
__device__ __forceinline__ u32x4 nz4(f32x4 v) { return u32x4{nz(v[0]), nz(v[1]), nz(v[2]), nz(v[3])}; }
__device__ __forceinline__ bool any_empty(u32x4 v) { return max(max(v[0], v[1]), max(v[2], v[3])) == EMPTY; }
__device__ __forceinline__ u32x4 tr2(LAS unsigned char* p, int o0, int o1) {
  const u32x2 a = __builtin_bit_cast(u32x2, __builtin_amdgcn_ds_read_tr16_b64_v4i16((LAS s16x4*)(p + o0)));
  const u32x2 b = __builtin_bit_cast(u32x2, __builtin_amdgcn_ds_read_tr16_b64_v4i16((LAS s16x4*)(p + o1)));
  return u32x4{a[0], a[1], b[0], b[1]};
}
__device__ __forceinline__ f32x4 gemv64(LAS unsigned char* img, LAS unsigned char* bp, int g, int r) {
  LAS unsigned char* const a = img + (8 * g + (r >> 2)) * 128 + (r & 3) * 8;
  const u32x4 b0 = *(LAS u32x4*)(bp + g * 16), b1 = *(LAS u32x4*)(bp + 64 + g * 16);
  f32x4 z[4];
#pragma unroll
  for (int n = 0; n < 4; ++n) {
    z[n] = mfma16(tr2(a, n * 32, n * 32 + 512), b0, f32x4{0.f, 0.f, 0.f, 0.f});
    z[n] = mfma16(tr2(a, 4096 + n * 32, 4096 + n * 32 + 512), b1, z[n]);
  }
  return (r & 4) ? ((r & 2) ? z[3] : z[2]) : ((r & 2) ? z[1] : z[0]);
}
#define BAR() asm volatile("s_waitcnt lgkmcnt(0)\n\ts_barrier" ::: "memory")
#define VM0() __builtin_amdgcn_s_waitcnt(0x0F70)
#define FENCE() asm volatile("" ::: "memory")
#define VMW(N) do { __builtin_amdgcn_s_waitcnt(0x0F70 | ((N) & 15) | (((N) >> 4) << 14)); SCHED(); } while (0)

__global__ void __launch_bounds__(512, 2)
fused_decode(const u16* a_x, const u16* a_k, const u16* a_v, const u16* a_wq, const u16* a_wo, const u16* a_w1, const u16* a_b1, const u16* a_w2, const u16* a_b2,
             u16* a_out, unsigned char* a_ws) {
  __shared__ __attribute__((aligned(16))) unsigned char smem[L_END];
  LAS unsigned char* const L = (LAS unsigned char*)smem;
#define ARGS() const CAS u64* kp_ = (const CAS u64*)__builtin_amdgcn_kernarg_segment_ptr(); asm volatile("" : "+s"(kp_)); \
  const u16* const x = (const u16*)kp_[0]; const u16* const Kc = (const u16*)kp_[1]; const u16* const Vc = (const u16*)kp_[2]; const u16* const wq = (const u16*)kp_[3]; \
  const u16* const wo = (const u16*)kp_[4]; const u16* const w1 = (const u16*)kp_[5]; const u16* const b1 = (const u16*)kp_[6]; const u16* const w2 = (const u16*)kp_[7]; \
  const u16* const b2 = (const u16*)kp_[8]; u16* const out = (u16*)kp_[9]; unsigned char* const ws = (unsigned char*)kp_[10]; \
  const auto rw = __builtin_amdgcn_make_buffer_rsrc(ws, 0, WS_END, RS3); \
  (void)x; (void)Kc; (void)Vc; (void)wq; (void)wo; (void)w1; (void)b1; (void)w2; (void)b2; (void)out; (void)rw
#define PROBE(k) do {} while (0)
  const int lane = threadIdx.x & 63;
  int wave = __builtin_amdgcn_readfirstlane(threadIdx.x >> 6);
  const int xcd = blockIdx.x & 7, j = blockIdx.x >> 3, lw = xcd * 32 + j;
  const int g = lane >> 4, r = lane & 15;
  const unsigned box = WS_BOX + lw * BOX;
  const int pr = (xcd & 3) * 32 + j, ab = (pr >> 3) & 1, akv = pr >> 4, sub = pr & 7;
  const int par = xcd >> 2;
  constexpr unsigned TSTR = 32768, WSTR = 8192, KVLEN = 524288, PSTR = 2048;
  float gate = 0.f, xres = 0.f;
  u64 gnext = 0;

  if (wave < 4) {
    ARGS();
    gnext = *(const CAS u64*)(ws + WS_G) + 1;
    u32x4 xr;
    __builtin_amdgcn_s_setprio(3);
    {
      const u64 qb = (u64)(wq + (j & 3) * 128 * 4096 + xcd * 512 + (j >> 2) * 64);
      const u32x4 rq = {(unsigned)qb, (unsigned)(qb >> 32) & 0xffffu, 127u * 8192u + 128u, (unsigned)RS3};
      const unsigned vo = (wave * 32 + (lane >> 3)) * 8192 + (lane & 7) * 16;
      asm volatile("s_mov_b32 m0, %3\n\ts_nop 3\n\tglobal_load_dwordx4 %0, %7, %8\n\t"
                   "buffer_load_dwordx4 %1, %2, 0 offen nt lds\n\t"
                   "s_add_u32 m0, m0, 0x400\n\ts_nop 0\n\tbuffer_load_dwordx4 %1, %2, %4 offen nt lds\n\t"
                   "s_add_u32 m0, m0, 0x400\n\ts_nop 0\n\tbuffer_load_dwordx4 %1, %2, %5 offen nt lds\n\t"
                   "s_add_u32 m0, m0, 0x400\n\ts_nop 0\n\tbuffer_load_dwordx4 %1, %2, %6 offen nt lds"
                   : "=&v"(xr)
                   : "v"(vo), "s"(rq), "s"((unsigned)(size_t)(L + L_WQ) + wave * 4096), "s"(0x10000u), "s"(0x20000u), "s"(0x30000u),
                     "v"((wave & 1) * 1024 + lane * 16), "s"(x) : "memory", "scc");
    }
    __builtin_amdgcn_s_setprio(0);
    if (wave == 0) STAMP(21);
    const size_t kvoff = ((size_t)(ab * 8 + akv) * SK + sub * 4096 + par * PSTR) * 128;
    const auto rk = __builtin_amdgcn_make_buffer_rsrc((void*)(Kc + kvoff), 0, KVLEN, RS3);
    const auto rv = __builtin_amdgcn_make_buffer_rsrc((void*)(Vc + kvoff), 0, KVLEN, RS3);
#define LINK 0
#define LINV 0
    constexpr unsigned KST = LINK == 1 ? 1024 : LINK == 2 ? 1024 : 64, VST = LINV ? 1024 : 256;
    const unsigned koff = LINK == 1 ? lane * 16 : LINK == 2 ? (r >> 2) * 256 + g * 64 + (r & 3) * 16 : r * 256 + g * 16, voff = LINV ? lane * 16 : g * 1024 + r * 16, wb = wave * WSTR;
    const unsigned rot = ROT;
    u32x4 kb[2][8], vb[2][8];
#define ISSUE(H, T) do { const unsigned t_ = (T) + ((H) >> 3), o_ = wb + (((t_ + rot) & 15u) | (t_ & 16u)) * TSTR + (((H) >> 1) & 1) * 4096u; \
    if (((H) & 4) == 0) { _Pragma("unroll") for (int i = 0; i < 2; ++i) kb[((H) >> 3) & 1][((H) & 3) * 2 + i] = __builtin_amdgcn_raw_buffer_load_b128(rk, koff + (((H) & 1) * 2 + i) * KST, o_, AUXK); } \
    else { _Pragma("unroll") for (int i = 0; i < 2; ++i) vb[((H) >> 3) & 1][((H) & 3) * 2 + i] = __builtin_amdgcn_raw_buffer_load_b128(rv, voff + (((H) & 1) * 2 + i) * VST, o_, AUXV); } \
    SCHED(); } while (0)
#define PRE(H) do { if ((H) == PQ) { \
      if (wave == 0) STAMP(24); \
      asm volatile("s_waitcnt vmcnt(%1)" : "+v"(xr) : "n"(4 + 2 * PQ)); \
      if (wave == 0) STAMP(25); \
      *(LAS u32x4*)(L + L_XN + (wave & 1) * 1024 + lane * 16) = ln8(xr); \
      if (wave == 0) STAMP(16); \
      asm volatile("s_waitcnt vmcnt(%0) lgkmcnt(0)\n\ts_barrier" :: "n"(2 * PQ) : "memory");   } \
    if ((H) >= PQ) VMW(2 * (PQ - 1)); ISSUE(H, 0); } while (0)
    SCHED();
    PRE(0); PRE(1); PRE(2); PRE(3); PRE(4); PRE(5); PRE(6); PRE(7);
    if (wave == 0) STAMP(22);
#define PRE2(H) do { if ((H) < LK) PRE(H); } while (0)
#define PRE3(H) do { if ((H) >= LK) PRE(H); } while (0)
    PRE2(8); PRE2(9); PRE2(10); PRE2(11); PRE2(12); PRE2(13); PRE2(14); PRE2(15);
    {
      const bool mlp = j < 28;
      const u64 wb0 = wave == 0 ? (u64)(w1 + xcd * 64 * 1792 + (mlp ? j : 0) * 64) : wave == 1 ? (u64)(w2 + (mlp ? j : 0) * 64 * 512 + xcd * 64)
                                : (u64)(wo + (xcd * 512 + (j >> 3) * 128 + (wave - 2) * 4) * 512 + (j & 7) * 64);
      const unsigned rs = wave == 0 ? 3584u : wave == 1 ? 1024u : 8192u;
      const unsigned s1 = wave == 0 ? 8u * 3584u : wave == 1 ? 8192u : 65536u;
      const unsigned s2 = wave == 0 ? 16u * 3584u : wave == 1 ? 16384u : 1024u;
      const unsigned len = wave < 2 ? (mlp ? 63u * rs + 128u : 0u) : 123u * 1024u + 128u;
      const u32x4 rd = {(unsigned)wb0, (unsigned)(wb0 >> 32) & 0xffffu, len, (unsigned)RS3};
      const unsigned vo = (lane >> 3) * rs + (lane & 7) * 16;
      const unsigned lb = (unsigned)(size_t)L + (wave == 0 ? L_W1 : wave == 1 ? L_W2 : L_WO + (wave - 2) * 8192);
#pragma unroll
      for (int i = 0; i < 4; ++i)
        asm volatile("s_waitcnt vmcnt(%5)\n\ts_mov_b32 m0, %2\n\ts_nop 0\n\tbuffer_load_dwordx4 %0, %1, %3 offen nt lds\n\t"
                     "s_add_u32 m0, m0, 0x400\n\ts_nop 0\n\tbuffer_load_dwordx4 %0, %1, %4 offen nt lds"
                     :: "v"(vo), "s"(rd), "s"(lb + i * 2048), "s"(i * s2), "s"(i * s2 + s1), "n"(2 * (PQ - 1)) : "memory", "scc");
    }
    if (wave == 0) STAMP(26);
    const unsigned qsrc = WS_QG + (akv * 2 + ab) * 16384 + wave * 1024 + lane * 16;
    u32x4 qv[4];
#define QLOOK() do { _Pragma("unroll") for (int k = 0; k < 4; ++k) qv[k] = __builtin_amdgcn_raw_buffer_load_b128(rw, qsrc + k * 4096, 0, AUX_SC1); SCHED(); } while (0)
    VMW(2 * (PQ - 1) - 2);
    QLOOK();
    PRE3(8); PRE3(9); PRE3(10); PRE3(11); PRE3(12); PRE3(13); PRE3(14); PRE3(15);
    if (wave == 0) STAMP(27);
    {
      const unsigned tl = (unsigned)gnext & 0xffu, th = (unsigned)(gnext >> 8);
#define QBAD() (__builtin_amdgcn_ballot_w64(((((qv[0][0] ^ tl) | (qv[0][2] ^ tl) | (qv[1][0] ^ tl) | (qv[1][2] ^ tl) | (qv[2][0] ^ tl) | (qv[2][2] ^ tl) | (qv[3][0] ^ tl) | (qv[3][2] ^ tl)) << 24) | \
                 (qv[0][1] ^ th) | (qv[0][3] ^ th) | (qv[1][1] ^ th) | (qv[1][3] ^ th) | (qv[2][1] ^ th) | (qv[2][3] ^ th) | (qv[3][1] ^ th) | (qv[3][3] ^ th)) != 0) != 0)
      if (__builtin_expect(QBAD(), 0)) {
#pragma unroll 1
        for (unsigned spin = 0; spin < SPIN_CAP; ++spin) { FENCE(); QLOOK(); if (!QBAD()) break; }
      }
      *(LAS unsigned*)(L + L_Q + wave * 256 + lane * 4) =
          cvtpk((__uint_as_float(qv[0][0] & 0xffffff00u) + __uint_as_float(qv[1][0] & 0xffffff00u)) + (__uint_as_float(qv[2][0] & 0xffffff00u) + __uint_as_float(qv[3][0] & 0xffffff00u)),
                (__uint_as_float(qv[0][2] & 0xffffff00u) + __uint_as_float(qv[1][2] & 0xffffff00u)) + (__uint_as_float(qv[2][2] & 0xffffff00u) + __uint_as_float(qv[3][2] & 0xffffff00u)));
    }
    if (wave == 0) STAMP(1);
    BAR();
    if (wave == 0) STAMP(2);
    u32x4 qf[4];
#pragma unroll
    for (int t = 0; t < 4; ++t) qf[t] = *(LAS u32x4*)(L + L_Q + (r & 3) * 256 + t * 64 + g * 16);
    float m = -INFINITY, l = 0.f;
    f32x4 acc[8];
#pragma unroll
    for (int e = 0; e < 8; ++e) acc[e] = f32x4{0.f, 0.f, 0.f, 0.f};
    u32x4 pf;
#define QK(S) do { \
    f32x4 d0 = {0.f, 0.f, 0.f, 0.f}, d1 = {0.f, 0.f, 0.f, 0.f}; \
    _Pragma("unroll") for (int t = 0; t < 4; ++t) { d0 = mfma16(kb[S][t], qf[t], d0); d1 = mfma16(kb[S][4 + t], qf[t], d1); } \
    const float mn = fmaxf(m, xrow_max(fmaxf(fmaxf(fmaxf(d0[0], d0[1]), fmaxf(d0[2], d0[3])), fmaxf(fmaxf(d1[0], d1[1]), fmaxf(d1[2], d1[3]))))); \
    if (__builtin_amdgcn_ballot_w64(mn != m) != 0) { const float al = __builtin_amdgcn_exp2f((m - mn) * C2); l *= al; \
      _Pragma("unroll") for (int e = 0; e < 8; ++e) acc[e] *= al; m = mn; } \
    const float mc = m * C2; float p[8]; \
    _Pragma("unroll") for (int i = 0; i < 4; ++i) { p[i] = __builtin_amdgcn_exp2f(fmaf(d0[i], C2, -mc)); p[4 + i] = __builtin_amdgcn_exp2f(fmaf(d1[i], C2, -mc)); } \
    l += ((p[0] + p[1]) + (p[2] + p[3])) + ((p[4] + p[5]) + (p[6] + p[7])); \
    pf = u32x4{cvtpk(p[0], p[1]), cvtpk(p[2], p[3]), cvtpk(p[4], p[5]), cvtpk(p[6], p[7])}; SCHED(); } while (0)
#define PV(S) do { \
    _Pragma("unroll") for (int w = 0; w < 4; ++w) { u32x4 lo, hi; \
      _Pragma("unroll") for (int jp = 0; jp < 4; ++jp) { lo[jp] = __builtin_amdgcn_perm(vb[S][2 * jp + 1][w], vb[S][2 * jp][w], 0x05040100u); hi[jp] = __builtin_amdgcn_perm(vb[S][2 * jp + 1][w], vb[S][2 * jp][w], 0x07060302u); } \
      acc[2 * w] = mfma16(lo, pf, acc[2 * w]); acc[2 * w + 1] = mfma16(hi, pf, acc[2 * w + 1]); } SCHED(); } while (0)
#define CATCH(H) do { if ((H) < FQ) ISSUE(16 + (H), 0); } while (0)
    QK(0); CATCH(0); CATCH(1); CATCH(2); CATCH(3);
    PV(0); CATCH(4); CATCH(5); CATCH(6); CATCH(7);
    QK(1); CATCH(8); CATCH(9); CATCH(10); CATCH(11);
    PV(1);
    if (wave == 0) STAMP(3);
#define STEP(H) do { VMW(2 * (FQ - 1)); ISSUE((H) + FQ, t); } while (0)
#define PACE(H) do {} while (0)
#define STEP4(H) do { PACE(H); STEP(H); STEP((H) + 1); STEP((H) + 2); STEP((H) + 3); } while (0)
#pragma unroll 1
    for (int t = 2; t < 16; t += 2) {
      STAMP(32 + wave * 8 + (t >> 1) - 1);
      STEP4(0);
      *(volatile LAS int*)(L + L_PRG + wave * 4) = t;
      QK(0);
      STEP4(4); PV(0);
      STEP4(8); QK(1);
      STEP4(12); PV(1);
    }
    if (wave == 0) STAMP(4);
    STAMP(32 + wave * 8 + 7);
    l = xrow_sum(l);
    if (r < 4) {
#pragma unroll
      for (int e = 0; e < 8; ++e) *(LAS f32x4*)(L + L_A + (wave * 4 + r) * A_STR + e * 64 + g * 16) = acc[e];
      *(LAS float*)(L + L_M + r * 16 + wave * 4) = m; *(LAS float*)(L + L_L + r * 16 + wave * 4) = l;
    }
  }
  asm volatile("" : "+s"(wave));
  if (wave >= 4) {
    __builtin_amdgcn_s_setprio(2);
    ARGS();
    gnext = *(const CAS u64*)(ws + WS_G) + 1;
    if (wave == 4) STAMP(15);
    __builtin_amdgcn_raw_buffer_store_b128(u32x4{EMPTY, EMPTY, EMPTY, EMPTY}, rw, box + (wave - 4) * 1024 + lane * 16, 0, AUX_SC1);
    *(volatile LAS int*)(L + L_PRG + (wave - 4) * 4) = 0;
    BAR();
    PROBE(1);
    if (wave == 4) STAMP(8);
    LAS unsigned char* const ap = L + L_WQ + (8 * g + (r >> 2)) * 128 + (r & 3) * 8 + (wave - 4) * 32;
    LAS unsigned char* const bp = L + L_XN + (r & 1) * 1024 + (j & 3) * 256 + g * 16;
    f32x4 qv = {0.f, 0.f, 0.f, 0.f};
#pragma unroll
    for (int k = 0; k < 4; ++k) qv = mfma16(tr2(ap, k * 4096, k * 4096 + 512), *(LAS u32x4*)(bp + k * 64), qv);
    const unsigned tl = (unsigned)gnext & 0xffu, th = (unsigned)(gnext >> 8);
    const unsigned q0 = __float_as_uint((r & 2) ? qv[2] : qv[0]) + 0x80u, q1 = __float_as_uint((r & 2) ? qv[3] : qv[1]) + 0x80u;
    __builtin_amdgcn_raw_buffer_store_b128(u32x4{(q0 & 0xffffff00u) | tl, th, (q1 & 0xffffff00u) | tl, th}, rw,
                                           r < 4 ? WS_QG + (xcd * 2 + (r & 1)) * 16384 + (j & 3) * 4096 + ((j >> 2) * 64 + (wave - 4) * 16 + g * 4 + (r & 2)) * 8 : OOB, 0, AUX_SC1);
    if (wave == 4) STAMP(9);
    asm volatile("s_waitcnt vmcnt(0)\n\ts_barrier" ::: "memory");
  }
  asm volatile("" : "+s"(wave));
  if (wave == 5) {
    ARGS();
    const unsigned tl = (unsigned)gnext, th = (unsigned)(gnext >> 32);
    __builtin_amdgcn_raw_buffer_store_b64(u32x2{tl, th}, rw, lane == 0 ? WS_CF + lw * 8 : OOB, 0, AUX_SC1);
    const bool mlp = j < 28;
    const int hcol = (lane >> 4) * 16 + (lane & 7) * 2;
    const unsigned bb = *(const GAS unsigned*)(b1 + (mlp ? j : 0) * 64 + hcol);
    const int ocol = xcd * 64 + (j & 15) * 4 + g;
    xres = blo(((const GAS u16*)x)[(lane & 1) * 512 + ocol]);
    const float bias = blo(((const GAS u16*)b2)[ocol]);
    SCHED();
    __builtin_amdgcn_s_setprio(0);
    *(LAS u32x4*)(L + L_Y + lane * 16) = ln8(*(LAS u32x4*)(L + L_XN + lane * 16));
    *(LAS u32x4*)(L + L_Y + 1024 + lane * 16) = ln8(*(LAS u32x4*)(L + L_XN + 1024 + lane * 16));
    SCHED();
#define AFTER(T) do { _Pragma("unroll 1") for (unsigned spin = 0; spin < SPIN_CAP; ++spin) { if (min(*(volatile LAS int*)(L + L_PRG), *(volatile LAS int*)(L + L_PRG + 4)) >= (T)) break; __builtin_amdgcn_s_sleep(16); } FENCE(); } while (0)
    AFTER(AT1);
#define MP 1
#define HI() do { SCHED(); __builtin_amdgcn_s_setprio(3); SCHED(); } while (0)
#define LO() do { SCHED(); if (MP) __builtin_amdgcn_s_setprio(0); SCHED(); } while (0)
    HI();
#pragma unroll 1
    for (unsigned spin = 0; spin < SPIN_CAP; ++spin) {
      FENCE();
      const u32x4 a = __builtin_amdgcn_raw_buffer_load_b128(rw, WS_CF + lane * 16, 0, AUX_SC1), c = __builtin_amdgcn_raw_buffer_load_b128(rw, WS_CF + 1024 + lane * 16, 0, AUX_SC1);
      const unsigned bad = (a[0] ^ tl) | (a[1] ^ th) | (a[2] ^ tl) | (a[3] ^ th) | (c[0] ^ tl) | (c[1] ^ th) | (c[2] ^ tl) | (c[3] ^ th);
      if (__builtin_amdgcn_ballot_w64(bad != 0) == 0) break;
      __builtin_amdgcn_s_sleep(16);
    }
    __builtin_amdgcn_raw_buffer_store_b64(u32x2{tl, th}, rw, (blockIdx.x == 0 && lane == 0) ? WS_G : OOB, 0, AUX_SC1);
    LO();
    STAMP(20);
    const unsigned o64 = r < 8 ? (r >> 1) * 128 + (r & 1) * 64 + g * 16 : OOB;
    if (mlp) {
      STAMP(17);
      const u32x4 hp = nz4(gemv64(L + L_W1, L + L_Y + (r & 1) * 1024 + xcd * 128, g, r));
      HI();
      __builtin_amdgcn_raw_buffer_store_b128(hp, rw, box + BX_HB + o64, 0, AUX_SC1);
      LO();
      STAMP(18);
      AFTER(AT2);
      HI();
      u32x2 v[8];
#pragma unroll 1
      for (unsigned spin = 0; spin < SPIN_CAP; ++spin) {
        FENCE();
        unsigned mx = 0;
#pragma unroll
        for (int k = 0; k < 8; ++k) v[k] = __builtin_amdgcn_raw_buffer_load_b64(rw, WS_BOX + (k * 32 + j) * BOX + BX_HB + lane * 8, 0, AUX_SC1);
#pragma unroll
        for (int k = 0; k < 8; ++k) mx = max(mx, max(v[k][0], v[k][1]));
        if (__builtin_amdgcn_ballot_w64(mx == EMPTY) == 0) break;
        __builtin_amdgcn_s_sleep(16);
      }
      LO();
      STAMP(19);
      float hh[2];
#pragma unroll
      for (int i = 0; i < 2; ++i) {
        float s = 0.f;
#pragma unroll
        for (int k = 0; k < 8; ++k) s += __uint_as_float(v[k][i]);
        const float z = rbf(rbf(s) + (i ? bhi(bb) : blo(bb)));
        const float u = 0.7978845608028654f * (z + 0.044715f * z * z * z);
        hh[i] = z * __builtin_amdgcn_rcpf(1.f + __builtin_amdgcn_exp2f(-2.f * LOG2E * u));
      }
      *(LAS unsigned*)(L + L_H + ((lane >> 3) & 1) * 128 + hcol * 2) = cvtpk(hh[0], hh[1]);
      const u32x4 zp = nz4(gemv64(L + L_W2, L + L_H + (r & 1) * 128, g, r));
      HI();
      __builtin_amdgcn_raw_buffer_store_b128(zp, rw, box + BX_ZB + o64, 0, AUX_SC1);
      LO();
      STAMP(11);
    }
    AFTER(AT3);
    HI();
    if (j < 16) {
      const unsigned src = lane < 56 ? WS_BOX + (xcd * 32 + (lane >> 1)) * BOX + BX_ZB + (j >> 2) * 128 + (lane & 1) * 64 + (j & 3) * 16 : OOB;
      u32x4 v;
#pragma unroll 1
      for (unsigned spin = 0; spin < SPIN_CAP; ++spin) {
        FENCE();
        v = __builtin_amdgcn_raw_buffer_load_b128(rw, src, 0, AUX_SC1);
        if (__builtin_amdgcn_ballot_w64(any_empty(v)) == 0) break;
        __builtin_amdgcn_s_sleep(8);
      }
      LO();
      float z = rows_reduce4(__builtin_bit_cast(f32x4, v));
      z += dpp<XOR2>(z); z += dpp<ROR4>(z); z += dpp<ROR8>(z);
      z = rbf(rbf(z) + bias);
      gate = rbf(z * __builtin_amdgcn_rcpf(1.f + __builtin_amdgcn_exp2f(-LOG2E * z)));
      STAMP(12);
    }
    HI();
  }
  asm volatile("" : "+s"(wave));
  ARGS();
  const unsigned psrc = WS_BOX + (xcd * 32 + (lane >> 2) * 2 + (wave >> 2)) * BOX + BX_PA + (j >> 3) * 512 + (wave & 3) * 128 + (lane & 3) * 16;
  const unsigned ssrc = WS_BOX + ((lane >> 3) * 32 + ((lane >> 1) & 3) * 8 + xcd) * BOX + BX_SB + ((j >> 2) & 3) * 128 + (lane & 1) * 64 + (j & 3) * 16;
  if (wave >= 6) {
    if (TOUCH_AT >= 0) {
#pragma unroll 1
      for (unsigned spin = 0; spin < SPIN_CAP; ++spin) { if (*(volatile LAS int*)(L + L_PRG) >= TOUCH_AT) break; __builtin_amdgcn_s_sleep(32); }
      const unsigned b0 = WS_BOX + (xcd * 32 + (lane >> 2) * 2 + (wave & 1)) * BOX + BX_PA + (j >> 3) * 512 + (lane & 3) * 128;
      const unsigned t0 = __builtin_amdgcn_raw_buffer_load_b32(rw, b0, 0, AUX_SC1), t1 = __builtin_amdgcn_raw_buffer_load_b32(rw, ssrc, 0, AUX_SC1);
      asm volatile("" :: "v"(t0), "v"(t1));
    }
  }

  BAR();
  PROBE(3);
  if (wave == 0) STAMP(5);
  if (wave == 4 || wave == 5) {
    const int t = (wave - 4) * 64 + lane, hh = t >> 5, d4 = t & 31;
    const f32x4 mm = *(LAS f32x4*)(L + L_M + hh * 16), ll = *(LAS f32x4*)(L + L_L + hh * 16);
    f32x4 a[4];
#pragma unroll
    for (int w = 0; w < 4; ++w) a[w] = *(LAS f32x4*)(L + L_A + (w * 4 + hh) * A_STR + d4 * 16);
    const float M = fmaxf(fmaxf(mm[0], mm[1]), fmaxf(mm[2], mm[3]));
    float wt[4];
#pragma unroll
    for (int w = 0; w < 4; ++w) wt[w] = __builtin_amdgcn_exp2f((mm[w] - M) * C2);
    const float Ls = (ll[0] * wt[0] + ll[1] * wt[1]) + (ll[2] * wt[2] + ll[3] * wt[3]);
    const f32x4 o = ((a[0] * wt[0] + a[1] * wt[1]) + (a[2] * wt[2] + a[3] * wt[3])) * __builtin_amdgcn_rcpf(Ls);
    const unsigned lse = __float_as_uint(fmaf(M, C2, __builtin_amdgcn_logf(Ls)));
    u32x4 e;
#pragma unroll
    for (int i = 0; i < 4; ++i) e[i] = __builtin_amdgcn_perm(min(__float_as_uint(o[i]), 0xFFFFFE00u) + 0x80u, lse, 0x07060500u + i);
    __builtin_amdgcn_raw_buffer_store_b128(e, rw, WS_BOX + (akv * 32 + sub * 4 + par * 2 + ab) * BOX + BX_PA + t * 16, 0, AUX_SC1);
    if (wave == 4) STAMP(13);
  }
  LAS unsigned char* const aw = L + L_WO + (8 * g + (r >> 2)) * 128 + (r & 3) * 8 + (wave & 3) * 32;
  u32x4 wa[4];
#pragma unroll
  for (int k = 0; k < 4; ++k) wa[k] = tr2(aw, k * 4096, k * 4096 + 512);
  {
    u32x4 v0, v1;
#pragma unroll 1
    for (unsigned spin = 0; spin < SPIN_CAP; ++spin) {
      FENCE();
      v0 = __builtin_amdgcn_raw_buffer_load_b128(rw, psrc, 0, AUX_SC1);
      v1 = __builtin_amdgcn_raw_buffer_load_b128(rw, psrc + 64, 0, AUX_SC1);
      if (__builtin_amdgcn_ballot_w64(any_empty(v0) || any_empty(v1)) == 0) break;
    }
    if (wave == 0) STAMP(6);
    const float lse = __uint_as_float(__builtin_amdgcn_perm(__builtin_amdgcn_perm(v0[3], v0[2], 0x04000000u), __builtin_amdgcn_perm(v0[1], v0[0], 0x00000400u), 0x07060100u));
    float M = fmaxf(lse, dpp<ROR4>(lse)); M = fmaxf(M, dpp<ROR8>(M)); M = xrow_max(M);
    const float wt = __builtin_amdgcn_exp2f(lse - M);
    float Ws = wt + dpp<ROR4>(wt); Ws += dpp<ROR8>(Ws); Ws = xrow_sum(Ws);
    float o0 = rows_reduce4(__builtin_bit_cast(f32x4, v0 & 0xFFFFFF00u) * wt), o1 = rows_reduce4(__builtin_bit_cast(f32x4, v1 & 0xFFFFFF00u) * wt);
    o0 += dpp<ROR4>(o0); o1 += dpp<ROR4>(o1); o0 += dpp<ROR8>(o0); o1 += dpp<ROR8>(o1);
    const float inv = __builtin_amdgcn_rcpf(Ws);
    const float o = ((lane & 4) ? o1 : o0) * inv;
    *(LAS u16*)(L + L_O + (wave >> 2) * 256 + ((wave & 3) * 32 + (lane & 4) * 4 + (lane & 3) * 4 + g) * 2) = (u16)cvtpk(o, o);
  }
  BAR();
  if (wave < 4) {
    f32x4 s = {0.f, 0.f, 0.f, 0.f};
#pragma unroll
    for (int k = 0; k < 4; ++k) s = mfma16(wa[k], *(LAS u32x4*)(L + L_O + (r & 1) * 256 + k * 64 + g * 16), s);
    __builtin_amdgcn_raw_buffer_store_b128(nz4(s), rw, r < 2 ? box + BX_SB + wave * 128 + r * 64 + g * 16 : OOB, 0, AUX_SC1);
    if (wave == 0) STAMP(14);
  }
  if (wave == 5 && j < 16) {
    u32x4 v;
#pragma unroll 1
    for (unsigned spin = 0; spin < SPIN_CAP; ++spin) {
      FENCE();
      v = __builtin_amdgcn_raw_buffer_load_b128(rw, ssrc, 0, AUX_SC1);
      if (__builtin_amdgcn_ballot_w64(any_empty(v)) == 0) break;
    }
    float s = rows_reduce4(__builtin_bit_cast(f32x4, v));
    s += dpp<XOR2>(s); s += dpp<ROR4>(s); s += dpp<ROR8>(s);
    const float res = xres + rbf(rbf(s) * gate);
    const auto rout = __builtin_amdgcn_make_buffer_rsrc(out, 0, 2048, RS3);
    __builtin_amdgcn_raw_buffer_store_b16((u16)cvtpk(res, res), rout, r < 2 ? (r * 512 + xcd * 64 + j * 4 + g) * 2 : OOB, 0, 0);
    STAMP(7);
  }
  PROBE(4);
}

extern "C" void kernel_launch(void* const* d_in, const int* in_sizes, int n_in, void* d_out, int out_size, void* d_ws, size_t ws_size, hipStream_t stream) {
  fused_decode<<<256, 512, 0, stream>>>((const u16*)d_in[0], (const u16*)d_in[1], (const u16*)d_in[2], (const u16*)d_in[3], (const u16*)d_in[4],
                                        (const u16*)d_in[6], (const u16*)d_in[7], (const u16*)d_in[8], (const u16*)d_in[9], (u16*)d_out, (unsigned char*)d_ws);
}
```

```cpp
#include <hip/hip_runtime.h>
#include <stdint.h>

typedef unsigned short u16;
typedef unsigned long long u64;
typedef unsigned u32x2 __attribute__((ext_vector_type(2)));
typedef unsigned u32x4 __attribute__((ext_vector_type(4)));
typedef float f32x2 __attribute__((ext_vector_type(2)));
typedef float f32x4 __attribute__((ext_vector_type(4)));
typedef short s16x4 __attribute__((ext_vector_type(4)));
typedef short bf16x8 __attribute__((ext_vector_type(8)));
typedef __bf16 bf16x2_t __attribute__((ext_vector_type(2)));
#define GAS __attribute__((address_space(1)))
#define LAS __attribute__((address_space(3)))
#define CAS __attribute__((address_space(4)))

#define WK 5
#define WE 8
#define TE 8
#define AH 6
#define PQ 5
#define P0 1
#define P1 5
#define LK 14
#define PD 16
#define PSL 4
static_assert(WK <= AH && AH <= 8 && WE <= 8 && TE % 2 == 0 && P0 < P1 && P1 <= PQ && PQ <= 8 && LK >= 8 && LK <= 16, "");

constexpr int SK = 32768;
constexpr int AUX_SC1 = 16, AUX_NT = 2, RS3 = 0x00020000;
constexpr unsigned WS_G = 0, WS_QG = 4096, WS_HG = WS_QG + 8 * 16384, WS_BOX = WS_HG + 224 * 512;
constexpr unsigned BOX = 3072 + 128, BX_PA = 0, BX_SB = 2048, BX_ZB = 2560, WS_END = WS_BOX + 256 * BOX;
constexpr int L_WQ = 0, L_WO = 16384, L_W1 = 32768, L_W2 = 40960, L_XN = 49152, L_Y = L_XN + 2048, L_Q = 53248, L_H = 54272, L_O = 54528,
              L_M = 55040, L_L = 55104, L_PRG = 55168, L_A = 55296, A_STR = 528, L_S = L_A + 16 * A_STR, L_END = L_S + 32768;
constexpr unsigned OOB = 0x80000000u;
constexpr unsigned SPIN_CAP = 1u << 19;
constexpr float LOG2E = 1.4426950408889634f, C2 = 0.08838834764831845f * LOG2E;

#define SCHED() __builtin_amdgcn_sched_barrier(0)

__device__ __forceinline__ f32x4 mfma16(u32x4 a, u32x4 b, f32x4 c) {
  return __builtin_amdgcn_mfma_f32_16x16x32_bf16(__builtin_bit_cast(bf16x8, a), __builtin_bit_cast(bf16x8, b), c, 0, 0, 0);
}
__device__ __forceinline__ unsigned cvtpk(float lo, float hi) { return __builtin_bit_cast(unsigned, __builtin_convertvector(f32x2{lo, hi}, bf16x2_t)); }
__device__ __forceinline__ float blo(unsigned u) { return __uint_as_float(u << 16); }
__device__ __forceinline__ float bhi(unsigned u) { return __uint_as_float(u & 0xffff0000u); }
__device__ __forceinline__ float rbf(float x) { return blo(cvtpk(x, x)); }
template <int CTRL> __device__ __forceinline__ float dpp(float x) {
  return __builtin_bit_cast(float, __builtin_amdgcn_mov_dpp(__builtin_bit_cast(int, x), CTRL, 0xf, 0xf, true));
}
constexpr int XOR1 = 0xB1, XOR2 = 0x4E, ROR4 = 0x124, ROR8 = 0x128, ROR12 = 0x12C, HMIR = 0x141;
__device__ __forceinline__ float max3(float a, float b, float c) { float d; asm("v_max3_f32 %0, %1, %2, %3" : "=v"(d) : "v"(a), "v"(b), "v"(c)); return d; }
__device__ __forceinline__ float xrow_max(float x) {
  auto s = __builtin_amdgcn_permlane16_swap(__float_as_uint(x), __float_as_uint(x), false, false);
  x = max3(__uint_as_float(s[0]), __uint_as_float(s[1]), __uint_as_float(s[1]));
  auto t = __builtin_amdgcn_permlane32_swap(__float_as_uint(x), __float_as_uint(x), false, false);
  return max3(__uint_as_float(t[0]), __uint_as_float(t[1]), __uint_as_float(t[1]));
}
__device__ __forceinline__ float xrow_sum(float x) {
  auto s = __builtin_amdgcn_permlane16_swap(__float_as_uint(x), __float_as_uint(x), false, false);
  x = __uint_as_float(s[0]) + __uint_as_float(s[1]);
  auto t = __builtin_amdgcn_permlane32_swap(__float_as_uint(x), __float_as_uint(x), false, false);
  return __uint_as_float(t[0]) + __uint_as_float(t[1]);
}
__device__ __forceinline__ float wave_sum(float x) {
  x += dpp<XOR1>(x); x += dpp<XOR2>(x); x += dpp<ROR4>(x); x += dpp<ROR8>(x); return xrow_sum(x);
}
__device__ __forceinline__ float rows_reduce4(f32x4 v) {
  auto a = __builtin_amdgcn_permlane32_swap(__float_as_uint(v[0]), __float_as_uint(v[2]), false, false);
  auto b = __builtin_amdgcn_permlane32_swap(__float_as_uint(v[1]), __float_as_uint(v[3]), false, false);
  const float u0 = __uint_as_float(a[0]) + __uint_as_float(a[1]), u1 = __uint_as_float(b[0]) + __uint_as_float(b[1]);
  auto c = __builtin_amdgcn_permlane16_swap(__float_as_uint(u0), __float_as_uint(u1), false, false);
  return __uint_as_float(c[0]) + __uint_as_float(c[1]);
}
__device__ __forceinline__ u32x4 ln8(u32x4 v) {
  float f[8];
#pragma unroll
  for (int i = 0; i < 4; ++i) { f[2 * i] = blo(v[i]); f[2 * i + 1] = bhi(v[i]); }
  const float mean = wave_sum(((f[0] + f[1]) + (f[2] + f[3])) + ((f[4] + f[5]) + (f[6] + f[7]))) * (1.f / 512.f);
  float ss = 0.f;
#pragma unroll
  for (int i = 0; i < 8; ++i) { const float d = f[i] - mean; ss = fmaf(d, d, ss); }
  const float var = wave_sum(ss) * (1.f / 512.f);
  const float mb = rbf(mean), inv = __builtin_amdgcn_rcpf(rbf(__builtin_amdgcn_sqrtf(rbf(rbf(var) + 1e-5f))));
  u32x4 o;
#pragma unroll
  for (int i = 0; i < 4; ++i) { const unsigned c = cvtpk(f[2 * i] - mb, f[2 * i + 1] - mb); o[i] = cvtpk(blo(c) * inv, bhi(c) * inv); }
  return o;
}
__device__ __forceinline__ u32x4 tr2(LAS unsigned char* p, int o0, int o1) {
  const u32x2 a = __builtin_bit_cast(u32x2, __builtin_amdgcn_ds_read_tr16_b64_v4i16((LAS s16x4*)(p + o0)));
  const u32x2 b = __builtin_bit_cast(u32x2, __builtin_amdgcn_ds_read_tr16_b64_v4i16((LAS s16x4*)(p + o1)));
  return u32x4{a[0], a[1], b[0], b[1]};
}
__device__ __forceinline__ f32x4 gemv64(LAS unsigned char* img, LAS unsigned char* bp, int g, int r) {
  LAS unsigned char* const a = img + (8 * g + (r >> 2)) * 128 + (r & 3) * 8;
  const u32x4 b0 = *(LAS u32x4*)(bp + g * 16), b1 = *(LAS u32x4*)(bp + 64 + g * 16);
  f32x4 z[4];
#pragma unroll
  for (int n = 0; n < 4; ++n) {
    z[n] = mfma16(tr2(a, n * 32, n * 32 + 512), b0, f32x4{0.f, 0.f, 0.f, 0.f});
    z[n] = mfma16(tr2(a, 4096 + n * 32, 4096 + n * 32 + 512), b1, z[n]);
  }
  return (r & 4) ? ((r & 2) ? z[3] : z[2]) : ((r & 2) ? z[1] : z[0]);
}
#define BAR() asm volatile("s_waitcnt lgkmcnt(0)\n\ts_barrier" ::: "memory")
#define FENCE() asm volatile("" ::: "memory")
#define VMW(N) do { __builtin_amdgcn_s_waitcnt(0x0F70 | ((N) & 15) | (((N) >> 4) << 14)); SCHED(); } while (0)

__device__ __forceinline__ unsigned rotr(unsigned x, unsigned n) { return __builtin_amdgcn_alignbit(x, x, n); }
__device__ __forceinline__ unsigned sum4(u32x4 w) { return w[0] + rotr(w[1], 8) + rotr(w[2], 16) + rotr(w[3], 24); }
__device__ __forceinline__ unsigned sum4_top(u32x4 w) {
  return (w[0] & 0xffffff00u) + __builtin_amdgcn_perm(0u, w[1], 0x0c030201u) + __builtin_amdgcn_perm(0u, w[2], 0x010c0302u) + __builtin_amdgcn_perm(0u, w[3], 0x02010c03u);
}
__device__ __forceinline__ u32x4 low_bytes(u32x4 w, unsigned d) {
  return u32x4{__builtin_amdgcn_perm(w[0], d, 0x07060500u), __builtin_amdgcn_perm(w[1], d, 0x07060503u), __builtin_amdgcn_perm(w[2], d, 0x07060502u), __builtin_amdgcn_perm(w[3], d, 0x07060501u)};
}
__device__ __forceinline__ u32x4 seal(f32x4 v, unsigned salt) { const u32x4 w = __builtin_bit_cast(u32x4, v); return low_bytes(w, salt - sum4_top(w)); }
__device__ __forceinline__ f32x4 vals(u32x4 w) { return __builtin_bit_cast(f32x4, w & 0xffffff00u); }
__device__ __forceinline__ f32x4 halves_sum(f32x4 v) {
  auto a = __builtin_amdgcn_permlane32_swap(__float_as_uint(v[0]), __float_as_uint(v[0]), false, false), b = __builtin_amdgcn_permlane32_swap(__float_as_uint(v[1]), __float_as_uint(v[1]), false, false);
  auto c = __builtin_amdgcn_permlane32_swap(__float_as_uint(v[2]), __float_as_uint(v[2]), false, false), d = __builtin_amdgcn_permlane32_swap(__float_as_uint(v[3]), __float_as_uint(v[3]), false, false);
  return f32x4{__uint_as_float(a[0]) + __uint_as_float(a[1]), __uint_as_float(b[0]) + __uint_as_float(b[1]), __uint_as_float(c[0]) + __uint_as_float(c[1]), __uint_as_float(d[0]) + __uint_as_float(d[1])};
}
#define SALT(g) ((unsigned)(g) * 0x9E3779B1u + 0x7F4A7C15u)
#define PIN(x) asm volatile("" : "+v"(x))

__global__ void __launch_bounds__(512, 2)
fused_decode(const u16* a_x, const u16* a_k, const u16* a_v, const u16* a_wq, const u16* a_wo, const u16* a_w1, const u16* a_b1, const u16* a_w2, const u16* a_b2,
             u16* a_out, unsigned char* a_ws) {
  __shared__ __attribute__((aligned(16))) unsigned char smem[L_END];
  LAS unsigned char* const L = (LAS unsigned char*)smem;
#define ARGS() const CAS u64* kp_ = (const CAS u64*)__builtin_amdgcn_kernarg_segment_ptr(); asm volatile("" : "+s"(kp_)); \
  const u16* const x = (const u16*)kp_[0]; const u16* const Kc = (const u16*)kp_[1]; const u16* const Vc = (const u16*)kp_[2]; const u16* const wq = (const u16*)kp_[3]; \
  const u16* const wo = (const u16*)kp_[4]; const u16* const w1 = (const u16*)kp_[5]; const u16* const b1 = (const u16*)kp_[6]; const u16* const w2 = (const u16*)kp_[7]; \
  const u16* const b2 = (const u16*)kp_[8]; u16* const out = (u16*)kp_[9]; unsigned char* const ws = (unsigned char*)kp_[10]; \
  const auto rw = __builtin_amdgcn_make_buffer_rsrc(ws, 0, WS_END, RS3); \
  (void)x; (void)Kc; (void)Vc; (void)wq; (void)wo; (void)w1; (void)b1; (void)w2; (void)b2; (void)out; (void)rw
  const int lane = threadIdx.x & 63;
  int wave = __builtin_amdgcn_readfirstlane(threadIdx.x >> 6);
  const int xcd = blockIdx.x & 7, j = blockIdx.x >> 3, lw = xcd * 32 + j;
  const int g = lane >> 4, r = lane & 15;
  const unsigned box = WS_BOX + lw * BOX;
  const int pr = (xcd & 3) * 32 + j, ab = (pr >> 3) & 1, akv = pr >> 4, slot = (pr & 7) * 2 + (xcd >> 2);
  const bool mlp = j < 28;
  const int jm = mlp ? j : 0;
  float xres = 0.f, bias2 = 0.f;
  u32x4 zp = {0u, 0u, 0u, 0u};
  unsigned salt = 0;
  u64 gnext = 0;

  if (wave < 4) {
    ARGS();
    asm volatile("s_setprio 3" ::: "memory");
    SCHED();
    unsigned vq = (wave * 32 + (lane >> 3)) * 8192 + (lane & 7) * 16, v1 = (wave * 16 + (lane >> 3)) * 3584 + (lane & 7) * 16;
    unsigned vx = (wave & 1) * 1024 + lane * 16;
    unsigned qo = ((j & 3) * 128 * 4096 + xcd * 512 + (j >> 2) * 64) * 2, o1 = (xcd * 64 * 1792 + jm * 64) * 2, len1 = mlp ? 63u * 3584u + 128u : 0u;
    constexpr unsigned PSTR = 8192, WSTR = 2048;
    unsigned koff = wave * WSTR + (r >> 2) * 256 + g * 64 + (r & 3) * 16, voff = wave * WSTR + lane * 16, kvo = ((ab * 8 + akv) * SK + slot * 2048) * 256u;
    asm volatile("" : "+v"(vx), "+v"(vq), "+v"(v1), "+v"(koff), "+v"(voff), "+s"(qo), "+s"(o1), "+s"(len1), "+s"(kvo));
    gnext = *(const CAS u64*)(ws + WS_G) + 1;
    {
      const u64 qb = (u64)wq + qo, b1_ = (u64)w1 + o1;
      const u32x4 rq = {(unsigned)qb, (unsigned)(qb >> 32) & 0xffffu, 127u * 8192u + 128u, (unsigned)RS3};
      const u32x4 r1 = {(unsigned)b1_, (unsigned)(b1_ >> 32) & 0xffffu, len1, (unsigned)RS3};
      const u32x4 rx = {(unsigned)(u64)x, (unsigned)((u64)x >> 32) & 0xffffu, 2048u, (unsigned)RS3};
      asm volatile("s_mov_b32 m0, %12\n\ts_nop 3\n\tbuffer_load_dwordx4 %10, %11, 0 offen lds\n\t"
                   "s_mov_b32 m0, %2\n\ts_nop 0\n\t"
                   "buffer_load_dwordx4 %0, %1, 0 offen nt lds\n\t"
                   "s_add_u32 m0, m0, 0x400\n\ts_nop 0\n\tbuffer_load_dwordx4 %0, %1, %3 offen nt lds\n\t"
                   "s_add_u32 m0, m0, 0x400\n\ts_nop 0\n\tbuffer_load_dwordx4 %0, %1, %4 offen nt lds\n\t"
                   "s_add_u32 m0, m0, 0x400\n\ts_nop 0\n\tbuffer_load_dwordx4 %0, %1, %5 offen nt lds\n\t"
                   "s_mov_b32 m0, %8\n\ts_nop 0\n\tbuffer_load_dwordx4 %6, %7, 0 offen nt lds\n\t"
                   "s_add_u32 m0, m0, 0x400\n\ts_nop 0\n\tbuffer_load_dwordx4 %6, %7, %9 offen nt lds"
                   :: "v"(vq), "s"(rq), "s"((unsigned)(size_t)(L + L_WQ) + wave * 4096), "s"(0x10000u), "s"(0x20000u), "s"(0x30000u),
                      "v"(v1), "s"(r1), "s"((unsigned)(size_t)(L + L_W1) + wave * 2048), "s"(8u * 3584u),
                      "v"(vx), "s"(rx), "s"((unsigned)(size_t)(L + L_XN) + wave * 1024) : "memory", "scc");
    }
    __builtin_amdgcn_s_setprio(0);
    const auto rk = __builtin_amdgcn_make_buffer_rsrc((char*)Kc + kvo, 0, 2048 * 256, RS3);
    const auto rv = __builtin_amdgcn_make_buffer_rsrc((char*)Vc + kvo, 0, 2048 * 256, RS3);
    u32x4 tb[4][8];
#define LOAD(B, I, RS, VO, T) do { tb[B][I] = __builtin_amdgcn_raw_buffer_load_b128(RS, (VO) + ((I) & 1) * 1024, (unsigned)(T) * 32768u + ((I) >> 1) * PSTR, AUX_NT); SCHED(); } while (0)
#define ISSUE(H, T) do { LOAD((H) >> 2, ((H) & 3) * 2, rk, koff, (H) >> 2); LOAD((H) >> 2, ((H) & 3) * 2 + 1, rk, koff, (H) >> 2); } while (0)
#define PRE(H) do { if ((H) == P0) asm volatile("s_waitcnt vmcnt(%0)\n\ts_barrier" :: "n"(6 + 2 * P0) : "memory");   \
    if ((H) == P1) { salt = SALT(gnext); asm volatile("s_waitcnt vmcnt(%1) lgkmcnt(0)\n\ts_barrier" : "+s"(salt) : "n"(2 * P1) : "memory"); }   \
    if ((H) >= PQ) VMW(2 * (PQ - 1)); ISSUE(H, 0); } while (0)
#define PRE2(H) do { if ((H) < LK) PRE(H); } while (0)
#define PRE3(H) do { if ((H) >= LK) PRE(H); } while (0)
    SCHED();
    PRE(0); PRE(1); PRE(2); PRE(3); PRE(4); PRE(5); PRE(6); PRE(7);
    PRE2(8); PRE2(9); PRE2(10); PRE2(11); PRE2(12); PRE2(13); PRE2(14); PRE2(15);
    const unsigned qsrc = WS_QG + akv * 16384 + (lane >> 5) * 4096 + (wave * 8 + ((lane >> 2) & 7)) * 128 + ab * 64 + (lane & 3) * 16;
    u32x4 qv[2];
    {
      const u32x4 rwq = {(unsigned)(u64)ws, (unsigned)((u64)ws >> 32) & 0xffffu, WS_END, (unsigned)RS3};
      asm volatile("s_waitcnt vmcnt(%5)\n\tbuffer_load_dwordx4 %0, %2, %3, 0 offen sc1\n\tbuffer_load_dwordx4 %1, %2, %3, %4 offen sc1"
                   : "=&v"(qv[0]), "=&v"(qv[1]) : "v"(qsrc), "s"(rwq), "s"(8192u), "n"(2 * (PQ - 1)) : "memory");
    }
    PRE3(8); PRE3(9); PRE3(10); PRE3(11); PRE3(12); PRE3(13); PRE3(14); PRE3(15);
    {
      const u64 b2_ = (u64)(w2 + jm * 64 * 512 + xcd * 64), bo = (u64)(wo + (xcd * 512 + (j >> 3) * 128) * 512 + (j & 7) * 64);
      const u32x4 r2 = {(unsigned)b2_, (unsigned)(b2_ >> 32) & 0xffffu, mlp ? 63u * 1024u + 128u : 0u, (unsigned)RS3};
      const u32x4 ro = {(unsigned)bo, (unsigned)(bo >> 32) & 0xffffu, 127u * 1024u + 128u, (unsigned)RS3};
#define DMA2(V, R, LB, S0, S1) asm volatile("s_waitcnt vmcnt(%5)\n\ts_mov_b32 m0, %2\n\ts_nop 0\n\tbuffer_load_dwordx4 %0, %1, %3 offen nt lds\n\t" \
                     "s_add_u32 m0, m0, 0x400\n\ts_nop 0\n\tbuffer_load_dwordx4 %0, %1, %4 offen nt lds" :: "v"(V), "s"(R), "s"(LB), "s"(S0), "s"(S1), "n"(2 * (PQ - 1)) : "memory", "scc")
      const unsigned v2 = (wave * 16 + (lane >> 3)) * 1024 + (lane & 7) * 16, vo = (lane >> 3) * 8192 + wave * 2048 + (lane & 7) * 16;
      DMA2(v2, r2, (unsigned)(size_t)(L + L_W2) + wave * 2048, 0u, 8192u);
      DMA2(vo, ro, (unsigned)(size_t)(L + L_WO) + wave * 4096, 0u, 65536u);
      DMA2(vo, ro, (unsigned)(size_t)(L + L_WO) + wave * 4096 + 2048, 1024u, 65536u + 1024u);
    }
    asm volatile("s_waitcnt vmcnt(%2)" : "+v"(qv[0]), "+v"(qv[1]) : "n"(2 * (16 - LK) + 6));
    {
#define QBAD() (__builtin_amdgcn_ballot_w64(((sum4(qv[0]) ^ salt) | (sum4(qv[1]) ^ salt)) != 0) != 0)
      if (__builtin_expect(QBAD(), 0)) {
#pragma unroll 1
        for (unsigned spin = 0; spin < SPIN_CAP; ++spin) {
          FENCE();
          qv[0] = __builtin_amdgcn_raw_buffer_load_b128(rw, qsrc, 0, AUX_SC1); qv[1] = __builtin_amdgcn_raw_buffer_load_b128(rw, qsrc + 8192, 0, AUX_SC1);
          if (!QBAD()) break;
        }
      }
      const f32x4 q4 = halves_sum(vals(qv[0]) + vals(qv[1]));
      *(LAS u32x2*)(L + L_Q + wave * 256 + (lane & 31) * 8) = u32x2{cvtpk(q4[0], q4[1]), cvtpk(q4[2], q4[3])};
    }
    BAR();
    const int head = (r & 3) ^ ((r & 4) ? 3 : 0);
    const u32x4 qd = *(LAS u32x4*)(L + L_Q + head * 256 + g * 64 + (r >> 2) * 16);
    LAS unsigned char* sr = L + L_S + wave * 8192 + g * 128 + head * 32;
    LAS unsigned char* sw = sr + (r & 4) * 4 + (r >> 3) * 4;
    float m = -3.0e38f, y[4];
#define QP(B, P) do { const f32x4 z4 = {0.f, 0.f, 0.f, 0.f}, da = mfma16(tb[B][2 * (P)], qd, z4), db = mfma16(tb[B][2 * (P) + 1], qd, z4); \
    const float a0 = (r & 4) ? da[1] : da[0], a1 = (r & 4) ? da[3] : da[2], b0 = (r & 4) ? db[1] : db[0], b1 = (r & 4) ? db[3] : db[2]; \
    y[P] = ((r & 8) ? b1 : a0) + dpp<ROR8>((r & 8) ? a1 : b0); } while (0)
#define QE(T) do { const float z0 = ((r & 4) ? y[2] : y[0]) + dpp<HMIR>((r & 4) ? y[0] : y[2]), z1 = ((r & 4) ? y[3] : y[1]) + dpp<HMIR>((r & 4) ? y[1] : y[3]); \
    m = fmaxf(m, fmaxf(z0, z1)); *(LAS float*)(sw + (T) * 512) = z0; *(LAS float*)(sw + (T) * 512 + 8) = z1; } while (0)
#define QS(B, T) do { QP(B, 0); QP(B, 1); QP(B, 2); QP(B, 3); QE(T); SCHED(); } while (0)
#define CATCH(I) do { if ((I) < AH) LOAD(0, I, rk, koff, 4); } while (0)
    QS(0, 0); CATCH(0); CATCH(1); CATCH(2); CATCH(3); CATCH(4); CATCH(5); CATCH(6); CATCH(7);
    QS(1, 1); QS(2, 2); QS(3, 3);
    VMW(AH);
    *(volatile LAS int*)(L + L_PRG + wave * 4) = 1;
    LAS unsigned char* la = L + L_A + (wave * 4 + r) * A_STR + g * 16;
    PIN(la);
#define STEP(H, A, W, RS, VO, RN, VN, TN) do { VMW((W) - 1); \
    if ((H) + (A) < 16) LOAD(((H) + (A)) >> 3, ((H) + (A)) & 7, RS, VO, t + (((H) + (A)) >> 3)); else LOAD(0, ((H) + (A)) & 7, RN, VN, TN); } while (0)
#define STEP2(H, ...) do { STEP(H, __VA_ARGS__); STEP((H) + 1, __VA_ARGS__); } while (0)
#define KT(B, ...) do { STEP2(8 * (B), __VA_ARGS__); QP(B, 0); SCHED(); STEP2(8 * (B) + 2, __VA_ARGS__); QP(B, 1); SCHED(); STEP2(8 * (B) + 4, __VA_ARGS__); QP(B, 2); SCHED(); \
    STEP2(8 * (B) + 6, __VA_ARGS__); QP(B, 3); QE(t + (B)); SCHED(); } while (0)
#pragma unroll 1
    for (int t = 4; t < 16; t += 2) {
      const bool last = t == 14;
      const auto rn = __builtin_amdgcn_make_buffer_rsrc((last ? (char*)Vc : (char*)Kc) + kvo, 0, 2048 * 256, RS3);
      const unsigned vn = last ? voff : koff, tn = (t + 2) & 15;
      KT(0, AH, WK, rk, koff, rn, vn, tn); KT(1, AH, WK, rk, koff, rn, vn, tn);
    }
    m = fmaxf(m, dpp<HMIR>(m)); m = fmaxf(m, dpp<ROR8>(m));
    const float mc = xrow_max(m) * C2;
    float l = 0.f;
    f32x4 acc[8];
#pragma unroll
    for (int e = 0; e < 8; ++e) acc[e] = f32x4{0.f, 0.f, 0.f, 0.f};
    u32x4 pf, vt[8];
#define SP(T) do { const f32x4 s0 = *(LAS f32x4*)(sr + (T) * 512), s1 = *(LAS f32x4*)(sr + (T) * 512 + 16); float p[8]; \
    _Pragma("unroll") for (int i = 0; i < 4; ++i) { p[i] = __builtin_amdgcn_exp2f(fmaf(s0[i], C2, -mc)); p[4 + i] = __builtin_amdgcn_exp2f(fmaf(s1[i], C2, -mc)); } \
    l += ((p[0] + p[1]) + (p[2] + p[3])) + ((p[4] + p[5]) + (p[6] + p[7])); \
    pf = u32x4{cvtpk(p[0], p[1]), cvtpk(p[2], p[3]), cvtpk(p[4], p[5]), cvtpk(p[6], p[7])}; SCHED(); } while (0)
#define VP(B, P) do { _Pragma("unroll") for (int w = 0; w < 4; ++w) { vt[2 * w][P] = __builtin_amdgcn_perm(tb[B][2 * (P) + 1][w], tb[B][2 * (P)][w], 0x05040100u); \
      vt[2 * w + 1][P] = __builtin_amdgcn_perm(tb[B][2 * (P) + 1][w], tb[B][2 * (P)][w], 0x07060302u); } } while (0)
#define VE() do { _Pragma("unroll") for (int e = 0; e < 8; ++e) acc[e] = mfma16(vt[e], pf, acc[e]); SCHED(); } while (0)
#define VT(B, ...) do { STEP2(8 * (B), __VA_ARGS__); VP(B, 0); SP(t + (B)); STEP2(8 * (B) + 2, __VA_ARGS__); VP(B, 1); SCHED(); STEP2(8 * (B) + 4, __VA_ARGS__); VP(B, 2); SCHED(); \
    STEP2(8 * (B) + 6, __VA_ARGS__); VP(B, 3); VE(); } while (0)
#pragma unroll 1
    for (int t = 0; t < TE; t += 2) { VT(0, AH, WK, rv, voff, rv, voff, t + 2); VT(1, AH, WK, rv, voff, rv, voff, t + 2); }
#define REST(I) do { if ((I) >= AH) LOAD(0, I, rv, voff, TE); } while (0)
    REST(4); REST(5); REST(6); REST(7);
#pragma unroll 1
    for (int t = TE; t < 16; t += 2) { VT(0, 8, WE, rv, voff, rv, voff, t + 2); VT(1, 8, WE, rv, voff, rv, voff, t + 2); }
    l = xrow_sum(l);
    if (r < 4) {
#pragma unroll
      for (int e = 0; e < 8; ++e) *(LAS f32x4*)(la + e * 64) = acc[e];
      *(LAS float*)(L + L_M + r * 16 + wave * 4) = mc; *(LAS float*)(L + L_L + r * 16 + wave * 4) = l;
    }
    BAR();
  }
  asm volatile("" : "+s"(wave));
  if (wave >= 4) {
    __builtin_amdgcn_s_setprio(2);
    ARGS();
    const int n = wave - 4;
    gnext = *(const CAS u64*)(ws + WS_G) + 1;
    *(volatile LAS int*)(L + L_PRG + n * 4) = 0;
    unsigned qdst = r < 2 ? WS_QG + xcd * 16384 + (j & 3) * 4096 + ((j >> 2) * 4 + n) * 128 + r * 64 + g * 16 : OOB;
    unsigned hdst = (r < 2 && mlp) ? WS_HG + (j * 8 + xcd) * 512 + n * 128 + r * 64 + g * 16 : OOB;
    PIN(qdst); PIN(hdst);
    asm volatile("s_barrier" ::: "memory");
    u32x4 xr = *(LAS u32x4*)(L + L_XN + n * 1024 + lane * 16);
#pragma unroll 1
    for (int it = n >> 1; it >= 0; --it) xr = ln8(xr);
    *(LAS u32x4*)(L + L_XN + n * 1024 + lane * 16) = xr;
    salt = SALT(gnext);
    asm volatile("s_waitcnt lgkmcnt(0)\n\ts_barrier" : "+s"(salt), "+s"(gnext) :: "memory");
    const int ao = (8 * g + (r >> 2)) * 128 + (r & 3) * 8 + n * 32;
    LAS unsigned char* const bp = L + L_XN + (r & 1) * 1024 + g * 16;
    f32x4 qv = {0.f, 0.f, 0.f, 0.f}, hv = {0.f, 0.f, 0.f, 0.f};
#pragma unroll
    for (int k = 0; k < 4; ++k) qv = mfma16(tr2(L + L_WQ + ao, k * 4096, k * 4096 + 512), *(LAS u32x4*)(bp + (j & 3) * 256 + k * 64), qv);
#pragma unroll
    for (int k = 0; k < 2; ++k) hv = mfma16(tr2(L + L_W1 + ao, k * 4096, k * 4096 + 512), *(LAS u32x4*)(bp + 2048 + xcd * 128 + k * 64), hv);
    __builtin_amdgcn_raw_buffer_store_b128(seal(qv, salt), rw, qdst, 0, AUX_SC1);
    __builtin_amdgcn_raw_buffer_store_b128(seal(hv, salt), rw, hdst, 0, AUX_SC1);
    asm volatile("s_barrier" ::: "memory");
  }
  asm volatile("" : "+s"(wave));
  if (wave == 6) {
    ARGS();
    __builtin_amdgcn_s_setprio(0);
#pragma unroll 1
    for (unsigned spin = 0; spin < SPIN_CAP; ++spin) {
      const u32x4 p = *(volatile LAS u32x4*)(L + L_PRG);
      if (min(min(p[0], p[1]), min(p[2], p[3])) != 0) break;
      __builtin_amdgcn_s_sleep(16);
    }
    SCHED();
    __builtin_amdgcn_s_setprio(3);
    const int hcol = ((lane >> 3) & 3) * 16 + (lane & 3) * 4;
    const u32x2 bb = *(const GAS u32x2*)(b1 + jm * 64 + hcol);
    const int ocol = xcd * 64 + (j & 15) * 4 + g;
    xres = blo(((const GAS u16*)x)[(lane & 1) * 512 + ocol]);
    bias2 = blo(((const GAS u16*)b2)[ocol]);
    u32x4 v[4];
#pragma unroll 1
    for (unsigned spin = 0; spin < SPIN_CAP; ++spin) {
      FENCE();
#pragma unroll
      for (int k = 0; k < 4; ++k) v[k] = __builtin_amdgcn_raw_buffer_load_b128(rw, WS_HG + jm * 4096 + k * 1024 + lane * 16, 0, AUX_SC1);
      if (__builtin_amdgcn_ballot_w64(((sum4(v[0]) ^ salt) | (sum4(v[1]) ^ salt) | (sum4(v[2]) ^ salt) | (sum4(v[3]) ^ salt)) != 0) == 0) break;
      __builtin_amdgcn_s_sleep(16);
    }
    SCHED();
    __builtin_amdgcn_s_setprio(0);
    const f32x4 hs = halves_sum((vals(v[0]) + vals(v[1])) + (vals(v[2]) + vals(v[3])));
    float hh[4];
#pragma unroll
    for (int i = 0; i < 4; ++i) {
      const float z = rbf(rbf(hs[i]) + ((i & 1) ? bhi(bb[i >> 1]) : blo(bb[i >> 1])));
      const float u = 0.7978845608028654f * (z + 0.044715f * z * z * z);
      hh[i] = z * __builtin_amdgcn_rcpf(1.f + __builtin_amdgcn_exp2f(-2.f * LOG2E * u));
    }
    *(LAS u32x2*)(L + L_H + ((lane >> 2) & 1) * 128 + hcol * 2) = u32x2{cvtpk(hh[0], hh[1]), cvtpk(hh[2], hh[3])};
    zp = seal(gemv64(L + L_W2, L + L_H + (r & 1) * 128, g, r), salt);
    PIN(zp); PIN(xres); PIN(bias2);
  }
  asm volatile("" : "+s"(wave));
  ARGS();
  if (wave >= 4) {
    unsigned dst1 = wave == 7 ? OOB : wave == 6 ? ((r < 8 && mlp) ? box + BX_ZB + (r >> 1) * 128 + (r & 1) * 64 + g * 16 : OOB)
                                                : WS_BOX + (akv * 32 + slot * 2 + ab) * BOX + BX_PA + ((wave & 1) * 64 + lane) * 16;
    const int hh = (wave & 1) * 2 + (lane >> 5);
    LAS unsigned char *lm = L + L_M + hh * 16, *la = L + L_A + hh * A_STR + (lane & 31) * 16;
    PIN(dst1); PIN(lm); PIN(la);
    asm volatile("" :: "s"(ws));
    BAR();
    if (wave < 6) {
      const f32x4 mm = *(LAS f32x4*)lm, ll = *(LAS f32x4*)(lm + (L_L - L_M));
      f32x4 a[4];
#pragma unroll
      for (int w = 0; w < 4; ++w) a[w] = *(LAS f32x4*)(la + w * 4 * A_STR);
      SCHED();
      const float M = max3(max3(mm[0], mm[1], mm[2]), mm[3], mm[3]);
      float wt[4];
#pragma unroll
      for (int w = 0; w < 4; ++w) wt[w] = __builtin_amdgcn_exp2f(mm[w] - M);
      const float Ls = fmaf(ll[3], wt[3], fmaf(ll[2], wt[2], fmaf(ll[1], wt[1], ll[0] * wt[0])));
      f32x4 o = a[0] * wt[0];
#pragma unroll
      for (int w = 1; w < 4; ++w) o = __builtin_elementwise_fma(a[w], f32x4{wt[w], wt[w], wt[w], wt[w]}, o);
      o *= __builtin_amdgcn_rcpf(Ls);
      const unsigned lse = __float_as_uint(M + __builtin_amdgcn_logf(Ls));
      const u32x4 w = __builtin_bit_cast(u32x4, o);
      const unsigned sw = sum4_top(w), so = (unsigned)__builtin_amdgcn_mov_dpp((int)sw, XOR1, 0xf, 0xf, true);
      zp = low_bytes(w, (lane & 1) ? lse : salt - sw - rotr(so + lse, 5));
    }
    __builtin_amdgcn_raw_buffer_store_b128(zp, rw, dst1, 0, AUX_SC1);
  }
  unsigned psrc = WS_BOX + (xcd * 32 + (lane >> 2) * 2 + (wave >> 2)) * BOX + BX_PA + (j >> 3) * 512 + (wave & 3) * 128 + (lane & 3) * 32;
  unsigned dst2 = r < 2 ? box + BX_SB + (wave & 3) * 128 + r * 64 + g * 16 : OOB;
  LAS unsigned char* lo = L + L_O + (wave >> 2) * 256 + ((wave & 3) * 32 + (lane & 3) * 8 + (lane & 4) + g) * 2;
  LAS unsigned char* lb = L + L_O + (r & 1) * 256 + g * 16;
  PIN(dst2); PIN(lo); PIN(lb);
  LAS unsigned char* const aw = L + L_WO + (8 * g + (r >> 2)) * 128 + (r & 3) * 8 + (wave & 3) * 32;
  u32x4 wa[4];
#pragma unroll
  for (int k = 0; k < 4; ++k) wa[k] = tr2(aw, k * 4096, k * 4096 + 512);
  {
    u32x4 v0, v1;
    int spin = SPIN_CAP;
    if (PD) __builtin_amdgcn_s_sleep(PD);
#pragma unroll 1
    do {
      FENCE();
      v0 = __builtin_amdgcn_raw_buffer_load_b128(rw, psrc, 0, AUX_SC1);
      v1 = __builtin_amdgcn_raw_buffer_load_b128(rw, psrc + 16, 0, AUX_SC1);
      if (__builtin_amdgcn_ballot_w64(sum4(v0) + rotr(sum4(v1), 5) != salt) == 0) break;
      if (PSL) __builtin_amdgcn_s_sleep(PSL);
      asm volatile("" : "+s"(spin));
    } while (--spin > 0);
    const float lse = __uint_as_float(__builtin_amdgcn_perm(__builtin_amdgcn_perm(v1[1], v1[2], 0x04000000u), __builtin_amdgcn_perm(v1[3], v1[0], 0x00000400u), 0x07060100u));
    float M = max3(lse, dpp<ROR4>(lse), dpp<ROR8>(lse)); M = max3(M, dpp<ROR12>(lse), M); M = xrow_max(M);
    const float wt = __builtin_amdgcn_exp2f(lse - M);
    float Ws = wt + dpp<ROR4>(wt); Ws += dpp<ROR8>(Ws); Ws = xrow_sum(Ws);
    float o0 = rows_reduce4(vals(v0) * wt), o1 = rows_reduce4(vals(v1) * wt);
    o0 += dpp<ROR4>(o0); o1 += dpp<ROR4>(o1); o0 += dpp<ROR8>(o0); o1 += dpp<ROR8>(o1);
    const float inv = __builtin_amdgcn_rcpf(Ws);
    const float o = ((lane & 4) ? o1 : o0) * inv;
    *(LAS u16*)lo = (u16)cvtpk(o, o);
  }
  BAR();
  if (wave < 4) {
    u32x4 ob[4];
#pragma unroll
    for (int k = 0; k < 4; ++k) ob[k] = *(LAS u32x4*)(lb + k * 64);
    SCHED();
    const f32x4 z4 = {0.f, 0.f, 0.f, 0.f};
    const f32x4 s = mfma16(wa[1], ob[1], mfma16(wa[0], ob[0], z4)) + mfma16(wa[3], ob[3], mfma16(wa[2], ob[2], z4));
    __builtin_amdgcn_raw_buffer_store_b128(seal(s, salt), rw, dst2, 0, AUX_SC1);
  }
  if (wave == 6 && j < 16) {
    const unsigned in = (j >> 2) * 128 + (lane & 1) * 64 + (j & 3) * 16;
    const unsigned zsrc = lane < 56 ? WS_BOX + (xcd * 32 + (lane >> 1)) * BOX + BX_ZB + in : OOB;
    const unsigned ssrc = WS_BOX + ((lane >> 3) * 32 + ((lane >> 1) & 3) * 8 + xcd) * BOX + BX_SB + in;
    u32x4 v;
    int spin = SPIN_CAP;
#pragma unroll 1
    do {
      FENCE();
      v = __builtin_amdgcn_raw_buffer_load_b128(rw, zsrc, 0, AUX_SC1);
      if (__builtin_amdgcn_ballot_w64(lane < 56 && sum4(v) != salt) == 0) break;
      asm volatile("" : "+s"(spin));
    } while (--spin > 0);
    float z = rows_reduce4(vals(v));
    z += dpp<XOR2>(z); z += dpp<ROR4>(z); z += dpp<ROR8>(z);
    z = rbf(rbf(z) + bias2);
    float gate = rbf(z * __builtin_amdgcn_rcpf(1.f + __builtin_amdgcn_exp2f(-LOG2E * z)));
    unsigned odst = r < 2 ? (r * 512 + xcd * 64 + j * 4 + g) * 2 : OOB;
    PIN(gate); PIN(odst);
    spin = SPIN_CAP;
#pragma unroll 1
    do {
      FENCE();
      v = __builtin_amdgcn_raw_buffer_load_b128(rw, ssrc, 0, AUX_SC1);
      if (__builtin_amdgcn_ballot_w64(sum4(v) != salt) == 0) break;
      asm volatile("" : "+s"(spin));
    } while (--spin > 0);
    float s = rows_reduce4(vals(v));
    s += dpp<XOR2>(s); s += dpp<ROR4>(s); s += dpp<ROR8>(s);
    const float res = xres + rbf(rbf(s) * gate);
    const auto rout = __builtin_amdgcn_make_buffer_rsrc(out, 0, 2048, RS3);
    __builtin_amdgcn_raw_buffer_store_b16((u16)cvtpk(res, res), rout, odst, 0, 0);
    __builtin_amdgcn_raw_buffer_store_b64(u32x2{(unsigned)gnext, (unsigned)(gnext >> 32)}, rw, (blockIdx.x == 0 && lane == 0) ? WS_G : OOB, 0, AUX_SC1);
  }
}

extern "C" void kernel_launch(void* const* d_in, const int* in_sizes, int n_in, void* d_out, int out_size, void* d_ws, size_t ws_size, hipStream_t stream) {
  fused_decode<<<256, 512, 0, stream>>>((const u16*)d_in[0], (const u16*)d_in[1], (const u16*)d_in[2], (const u16*)d_in[3], (const u16*)d_in[4],
                                        (const u16*)d_in[6], (const u16*)d_in[7], (const u16*)d_in[8], (const u16*)d_in[9], (u16*)d_out, (unsigned char*)d_ws);
}
```

```cpp
#include <hip/hip_runtime.h>
#include <stdint.h>

typedef unsigned short u16;
typedef unsigned long long u64;
typedef unsigned u32x2 __attribute__((ext_vector_type(2)));
typedef unsigned u32x4 __attribute__((ext_vector_type(4)));
typedef float f32x2 __attribute__((ext_vector_type(2)));
typedef float f32x4 __attribute__((ext_vector_type(4)));
typedef short s16x4 __attribute__((ext_vector_type(4)));
typedef short bf16x8 __attribute__((ext_vector_type(8)));
typedef __bf16 bf16x2_t __attribute__((ext_vector_type(2)));
#define GAS __attribute__((address_space(1)))
#define LAS __attribute__((address_space(3)))
#define CAS __attribute__((address_space(4)))

#define WK 5
#define WE 8
#define TE 8
#define AH 6
#define PW 10
#define P0 1
#define LK 28
#define PD 18
#define PSL 2
static_assert(WK <= AH && AH <= 8 && WE <= 8 && TE % 2 == 0 && P0 < PW - 1 && PW <= 16 && LK >= 16 && LK <= 32, "");

constexpr int SK = 32768;
constexpr int AUX_SC1 = 16, AUX_NT = 2, RS3 = 0x00020000;
constexpr unsigned WS_G = 0, WS_QG = 4096, WS_HG = WS_QG + 8 * 16384, WS_BOX = WS_HG + 224 * 512;
constexpr unsigned BOX = 3072 + 128, BX_PA = 0, BX_SB = 2048, BX_ZB = 2560, WS_END = WS_BOX + 256 * BOX;
constexpr int L_WQ = 0, L_WO = 16384, L_W1 = 32768, L_W2 = 40960, L_XN = 49152, L_Y = L_XN + 2048, L_Q = 53248, L_H = 54272, L_O = 54528,
              L_M = 55040, L_L = 55104, L_PRG = 55168, L_F1 = 55184, L_A = 55296, A_STR = 528, L_S = L_A + 16 * A_STR, L_END = L_S + 32768;
constexpr unsigned OOB = 0x80000000u;
constexpr unsigned SPIN_CAP = 1u << 19;
constexpr float LOG2E = 1.4426950408889634f, C2 = 0.08838834764831845f * LOG2E;

#define SCHED() __builtin_amdgcn_sched_barrier(0)

__device__ __forceinline__ f32x4 mfma16(u32x4 a, u32x4 b, f32x4 c) {
  return __builtin_amdgcn_mfma_f32_16x16x32_bf16(__builtin_bit_cast(bf16x8, a), __builtin_bit_cast(bf16x8, b), c, 0, 0, 0);
}
__device__ __forceinline__ unsigned cvtpk(float lo, float hi) { return __builtin_bit_cast(unsigned, __builtin_convertvector(f32x2{lo, hi}, bf16x2_t)); }
__device__ __forceinline__ float blo(unsigned u) { return __uint_as_float(u << 16); }
__device__ __forceinline__ float bhi(unsigned u) { return __uint_as_float(u & 0xffff0000u); }
__device__ __forceinline__ float rbf(float x) { return blo(cvtpk(x, x)); }
template <int CTRL> __device__ __forceinline__ float dpp(float x) {
  return __builtin_bit_cast(float, __builtin_amdgcn_mov_dpp(__builtin_bit_cast(int, x), CTRL, 0xf, 0xf, true));
}
constexpr int XOR1 = 0xB1, XOR2 = 0x4E, ROR4 = 0x124, ROR8 = 0x128, HMIR = 0x141, ODD = 0xF5;
__device__ __forceinline__ float max3(float a, float b, float c) { float d; asm("v_max3_f32 %0, %1, %2, %3" : "=v"(d) : "v"(a), "v"(b), "v"(c)); return d; }
__device__ __forceinline__ float xrow_max(float x) {
  auto s = __builtin_amdgcn_permlane16_swap(__float_as_uint(x), __float_as_uint(x), false, false);
  x = max3(__uint_as_float(s[0]), __uint_as_float(s[1]), __uint_as_float(s[1]));
  auto t = __builtin_amdgcn_permlane32_swap(__float_as_uint(x), __float_as_uint(x), false, false);
  return max3(__uint_as_float(t[0]), __uint_as_float(t[1]), __uint_as_float(t[1]));
}
__device__ __forceinline__ float xrow_sum(float x) {
  auto s = __builtin_amdgcn_permlane16_swap(__float_as_uint(x), __float_as_uint(x), false, false);
  x = __uint_as_float(s[0]) + __uint_as_float(s[1]);
  auto t = __builtin_amdgcn_permlane32_swap(__float_as_uint(x), __float_as_uint(x), false, false);
  return __uint_as_float(t[0]) + __uint_as_float(t[1]);
}
__device__ __forceinline__ float wave_sum(float x) {
  x += dpp<XOR1>(x); x += dpp<XOR2>(x); x += dpp<ROR4>(x); x += dpp<ROR8>(x); return xrow_sum(x);
}
__device__ __forceinline__ float rows_reduce4(f32x4 v) {
  auto a = __builtin_amdgcn_permlane32_swap(__float_as_uint(v[0]), __float_as_uint(v[2]), false, false);
  auto b = __builtin_amdgcn_permlane32_swap(__float_as_uint(v[1]), __float_as_uint(v[3]), false, false);
  const float u0 = __uint_as_float(a[0]) + __uint_as_float(a[1]), u1 = __uint_as_float(b[0]) + __uint_as_float(b[1]);
  auto c = __builtin_amdgcn_permlane16_swap(__float_as_uint(u0), __float_as_uint(u1), false, false);
  return __uint_as_float(c[0]) + __uint_as_float(c[1]);
}
__device__ __forceinline__ u32x4 ln8(u32x4 v) {
  float f[8];
#pragma unroll
  for (int i = 0; i < 4; ++i) { f[2 * i] = blo(v[i]); f[2 * i + 1] = bhi(v[i]); }
  const float mean = wave_sum(((f[0] + f[1]) + (f[2] + f[3])) + ((f[4] + f[5]) + (f[6] + f[7]))) * (1.f / 512.f);
  float ss = 0.f;
#pragma unroll
  for (int i = 0; i < 8; ++i) { const float d = f[i] - mean; ss = fmaf(d, d, ss); }
  const float var = wave_sum(ss) * (1.f / 512.f);
  const float mb = rbf(mean), inv = __builtin_amdgcn_rcpf(rbf(__builtin_amdgcn_sqrtf(rbf(rbf(var) + 1e-5f))));
  u32x4 o;
#pragma unroll
  for (int i = 0; i < 4; ++i) { const unsigned c = cvtpk(f[2 * i] - mb, f[2 * i + 1] - mb); o[i] = cvtpk(blo(c) * inv, bhi(c) * inv); }
  return o;
}
__device__ __forceinline__ u32x4 tr2(LAS unsigned char* p, int o0, int o1) {
  const u32x2 a = __builtin_bit_cast(u32x2, __builtin_amdgcn_ds_read_tr16_b64_v4i16((LAS s16x4*)(p + o0)));
  const u32x2 b = __builtin_bit_cast(u32x2, __builtin_amdgcn_ds_read_tr16_b64_v4i16((LAS s16x4*)(p + o1)));
  return u32x4{a[0], a[1], b[0], b[1]};
}
__device__ __forceinline__ f32x4 gemv64(LAS unsigned char* img, LAS unsigned char* bp, int g, int r) {
  LAS unsigned char* const a = img + (8 * g + (r >> 2)) * 128 + (r & 3) * 8;
  const u32x4 b0 = *(LAS u32x4*)(bp + g * 16), b1 = *(LAS u32x4*)(bp + 64 + g * 16);
  f32x4 z[4];
#pragma unroll
  for (int n = 0; n < 4; ++n) {
    z[n] = mfma16(tr2(a, n * 32, n * 32 + 512), b0, f32x4{0.f, 0.f, 0.f, 0.f});
    z[n] = mfma16(tr2(a, 4096 + n * 32, 4096 + n * 32 + 512), b1, z[n]);
  }
  return (r & 4) ? ((r & 2) ? z[3] : z[2]) : ((r & 2) ? z[1] : z[0]);
}
#define BAR() asm volatile("s_waitcnt lgkmcnt(0)\n\ts_barrier" ::: "memory")
#define FENCE() asm volatile("" ::: "memory")
#define VMW(N) do { __builtin_amdgcn_s_waitcnt(0x0F70 | ((N) & 15) | (((N) >> 4) << 14)); SCHED(); } while (0)

__device__ __forceinline__ unsigned rotr(unsigned x, unsigned n) { return __builtin_amdgcn_alignbit(x, x, n); }
__device__ __forceinline__ unsigned sum4(u32x4 w) { return w[0] + rotr(w[1], 8) + rotr(w[2], 16) + rotr(w[3], 24); }
__device__ __forceinline__ unsigned sum4_top(u32x4 w) {
  return (w[0] & 0xffffff00u) + __builtin_amdgcn_perm(0u, w[1], 0x0c030201u) + __builtin_amdgcn_perm(0u, w[2], 0x010c0302u) + __builtin_amdgcn_perm(0u, w[3], 0x02010c03u);
}
__device__ __forceinline__ u32x4 low_bytes(u32x4 w, unsigned d) {
  return u32x4{__builtin_amdgcn_perm(w[0], d, 0x07060500u), __builtin_amdgcn_perm(w[1], d, 0x07060503u), __builtin_amdgcn_perm(w[2], d, 0x07060502u), __builtin_amdgcn_perm(w[3], d, 0x07060501u)};
}
__device__ __forceinline__ u32x4 seal(f32x4 v, unsigned salt) { const u32x4 w = __builtin_bit_cast(u32x4, v); return low_bytes(w, salt - sum4_top(w)); }
__device__ __forceinline__ f32x4 vals(u32x4 w) { return __builtin_bit_cast(f32x4, w & 0xffffff00u); }
__device__ __forceinline__ f32x4 halves_sum(f32x4 v) {
  auto a = __builtin_amdgcn_permlane32_swap(__float_as_uint(v[0]), __float_as_uint(v[0]), false, false), b = __builtin_amdgcn_permlane32_swap(__float_as_uint(v[1]), __float_as_uint(v[1]), false, false);
  auto c = __builtin_amdgcn_permlane32_swap(__float_as_uint(v[2]), __float_as_uint(v[2]), false, false), d = __builtin_amdgcn_permlane32_swap(__float_as_uint(v[3]), __float_as_uint(v[3]), false, false);
  return f32x4{__uint_as_float(a[0]) + __uint_as_float(a[1]), __uint_as_float(b[0]) + __uint_as_float(b[1]), __uint_as_float(c[0]) + __uint_as_float(c[1]), __uint_as_float(d[0]) + __uint_as_float(d[1])};
}
#define SALT(g) ((unsigned)(g) * 0x9E3779B1u + 0x7F4A7C15u)
#define GIVE_UP(mask) do { --spin; mask = spin > 0 ? mask : 0u; asm volatile("" : "+v"(mask), "+v"(spin)); } while (0)
#define PIN(x) asm volatile("" : "+v"(x))

__global__ void __launch_bounds__(512, 2)
fused_decode(const u16* a_x, const u16* a_k, const u16* a_v, const u16* a_wq, const u16* a_wo, const u16* a_w1, const u16* a_b1, const u16* a_w2, const u16* a_b2,
             u16* a_out, unsigned char* a_ws) {
  __shared__ __attribute__((aligned(16))) unsigned char smem[L_END];
  LAS unsigned char* const L = (LAS unsigned char*)smem;
#define ARGS() const CAS u64* kp_ = (const CAS u64*)__builtin_amdgcn_kernarg_segment_ptr(); asm volatile("" : "+s"(kp_)); \
  const u16* const x = (const u16*)kp_[0]; const u16* const Kc = (const u16*)kp_[1]; const u16* const Vc = (const u16*)kp_[2]; const u16* const wq = (const u16*)kp_[3]; \
  const u16* const wo = (const u16*)kp_[4]; const u16* const w1 = (const u16*)kp_[5]; const u16* const b1 = (const u16*)kp_[6]; const u16* const w2 = (const u16*)kp_[7]; \
  const u16* const b2 = (const u16*)kp_[8]; u16* const out = (u16*)kp_[9]; unsigned char* const ws = (unsigned char*)kp_[10]; \
  const auto rw = __builtin_amdgcn_make_buffer_rsrc(ws, 0, WS_END, RS3); \
  (void)x; (void)Kc; (void)Vc; (void)wq; (void)wo; (void)w1; (void)b1; (void)w2; (void)b2; (void)out; (void)rw
  const int lane = threadIdx.x & 63;
  int wave = __builtin_amdgcn_readfirstlane(threadIdx.x >> 6);
  const int xcd = blockIdx.x & 7, j = blockIdx.x >> 3, lw = xcd * 32 + j;
  const int g = lane >> 4, r = lane & 15;
  const unsigned box = WS_BOX + lw * BOX;
  const int pr = (xcd & 3) * 32 + j, ab = (pr >> 3) & 1, akv = pr >> 4, slot = (pr & 7) * 2 + (xcd >> 2);
  const bool mlp = j < 28;
  const int jm = mlp ? j : 0;
  float xres = 0.f, bias2 = 0.f;
  u32x4 zp = {0u, 0u, 0u, 0u};
  unsigned salt = 0;
  u64 gnext = 0;

  if (wave < 4) {
    ARGS();
    asm volatile("s_setprio 3" ::: "memory");
    SCHED();
    unsigned vq = (wave * 32 + (lane >> 3)) * 8192 + (lane & 7) * 16, v1 = (wave * 16 + (lane >> 3)) * 3584 + (lane & 7) * 16;
    unsigned vx = (wave & 1) * 1024 + lane * 16;
    unsigned qo = ((j & 3) * 128 * 4096 + xcd * 512 + (j >> 2) * 64) * 2, o1 = (xcd * 64 * 1792 + jm * 64) * 2, len1 = mlp ? 63u * 3584u + 128u : 0u;
    constexpr unsigned PSTR = 8192, WSTR = 2048;
    unsigned koff = wave * WSTR + (r >> 2) * 256 + g * 64 + (r & 3) * 16, voff = wave * WSTR + lane * 16, kvo = ((ab * 8 + akv) * SK + slot * 2048) * 256u;
    asm volatile("" : "+v"(vx), "+v"(vq), "+v"(v1), "+v"(koff), "+v"(voff), "+s"(qo), "+s"(o1), "+s"(len1), "+s"(kvo));
    {
      const u64 qb = (u64)wq + qo, b1_ = (u64)w1 + o1;
      const u32x4 rq = {(unsigned)qb, (unsigned)(qb >> 32) & 0xffffu, 127u * 8192u + 128u, (unsigned)RS3};
      const u32x4 r1 = {(unsigned)b1_, (unsigned)(b1_ >> 32) & 0xffffu, len1, (unsigned)RS3};
      const u32x4 rx = {(unsigned)(u64)x, (unsigned)((u64)x >> 32) & 0xffffu, 2048u, (unsigned)RS3};
      asm volatile("s_mov_b32 m0, %12\n\ts_nop 3\n\tbuffer_load_dwordx4 %10, %11, 0 offen lds\n\t"
                   "s_mov_b32 m0, %2\n\ts_nop 0\n\t"
                   "buffer_load_dwordx4 %0, %1, 0 offen nt lds\n\t"
                   "s_add_u32 m0, m0, 0x400\n\ts_nop 0\n\tbuffer_load_dwordx4 %0, %1, %3 offen nt lds\n\t"
                   "s_add_u32 m0, m0, 0x400\n\ts_nop 0\n\tbuffer_load_dwordx4 %0, %1, %4 offen nt lds\n\t"
                   "s_add_u32 m0, m0, 0x400\n\ts_nop 0\n\tbuffer_load_dwordx4 %0, %1, %5 offen nt lds\n\t"
                   "s_mov_b32 m0, %8\n\ts_nop 0\n\tbuffer_load_dwordx4 %6, %7, 0 offen nt lds\n\t"
                   "s_add_u32 m0, m0, 0x400\n\ts_nop 0\n\tbuffer_load_dwordx4 %6, %7, %9 offen nt lds"
                   :: "v"(vq), "s"(rq), "s"((unsigned)(size_t)(L + L_WQ) + wave * 4096), "s"(0x10000u), "s"(0x20000u), "s"(0x30000u),
                      "v"(v1), "s"(r1), "s"((unsigned)(size_t)(L + L_W1) + wave * 2048), "s"(8u * 3584u),
                      "v"(vx), "s"(rx), "s"((unsigned)(size_t)(L + L_XN) + wave * 1024) : "memory", "scc");
    }
    __builtin_amdgcn_s_setprio(0);
    const CAS u64* gp = (const CAS u64*)(ws + WS_G);
    asm volatile("" : "+s"(gp));
    const u64 gprev = *gp;
    const auto rk = __builtin_amdgcn_make_buffer_rsrc((char*)Kc + kvo, 0, 2048 * 256, RS3);
    const auto rv = __builtin_amdgcn_make_buffer_rsrc((char*)Vc + kvo, 0, 2048 * 256, RS3);
    u32x4 tb[4][8];
#define LOAD(B, I, RS, VO, T) do { tb[B][I] = __builtin_amdgcn_raw_buffer_load_b128(RS, (VO) + ((I) & 1) * 1024, (unsigned)(T) * 32768u + ((I) >> 1) * PSTR, AUX_NT); SCHED(); } while (0)
#define PRE(H) do { if ((H) == P0) asm volatile("s_waitcnt vmcnt(%0)\n\ts_barrier" :: "n"(6 + P0) : "memory");   \
    if ((H) > P0) VMW(PW - 1); \
    if ((H) == PW - 1) { FENCE(); *(volatile LAS int*)(L + L_F1 + wave * 4) = 1; SCHED(); }     \
    LOAD((H) >> 3, (H) & 7, rk, koff, (H) >> 3); } while (0)
#define PRE2(H) do { if ((H) < LK) PRE(H); } while (0)
#define PRE3(H) do { if ((H) >= LK) PRE(H); } while (0)
#define X8(M, H) do { M(H); M((H) + 1); M((H) + 2); M((H) + 3); M((H) + 4); M((H) + 5); M((H) + 6); M((H) + 7); } while (0)
    SCHED();
    X8(PRE, 0); X8(PRE, 8);
    { u64 g_ = gprev; asm volatile("" : "+s"(g_)); salt = SALT(g_ + 1); }
    X8(PRE2, 16); X8(PRE2, 24);
    const unsigned qsrc = WS_QG + akv * 16384 + (lane >> 5) * 4096 + (wave * 8 + ((lane >> 2) & 7)) * 128 + ab * 64 + (lane & 3) * 16;
    u32x4 qv[2];
    {
      const u32x4 rwq = {(unsigned)(u64)ws, (unsigned)((u64)ws >> 32) & 0xffffu, WS_END, (unsigned)RS3};
      asm volatile("s_waitcnt vmcnt(%5)\n\tbuffer_load_dwordx4 %0, %2, %3, 0 offen sc1\n\tbuffer_load_dwordx4 %1, %2, %3, %4 offen sc1"
                   : "=&v"(qv[0]), "=&v"(qv[1]) : "v"(qsrc), "s"(rwq), "s"(8192u), "n"(PW - 2) : "memory");
    }
    X8(PRE3, 16); X8(PRE3, 24);
    {
      const u64 b2_ = (u64)(w2 + jm * 64 * 512 + xcd * 64), bo = (u64)(wo + (xcd * 512 + (j >> 3) * 128) * 512 + (j & 7) * 64);
      const u32x4 r2 = {(unsigned)b2_, (unsigned)(b2_ >> 32) & 0xffffu, mlp ? 63u * 1024u + 128u : 0u, (unsigned)RS3};
      const u32x4 ro = {(unsigned)bo, (unsigned)(bo >> 32) & 0xffffu, 127u * 1024u + 128u, (unsigned)RS3};
#define DMA2(V, R, LB, S0, S1) asm volatile("s_waitcnt vmcnt(%5)\n\ts_mov_b32 m0, %2\n\ts_nop 0\n\tbuffer_load_dwordx4 %0, %1, %3 offen nt lds\n\t" \
                     "s_add_u32 m0, m0, 0x400\n\ts_waitcnt vmcnt(%5)\n\tbuffer_load_dwordx4 %0, %1, %4 offen nt lds" :: "v"(V), "s"(R), "s"(LB), "s"(S0), "s"(S1), "n"(PW - 1) : "memory", "scc")
      const unsigned v2 = (wave * 16 + (lane >> 3)) * 1024 + (lane & 7) * 16, vo = (lane >> 3) * 8192 + wave * 2048 + (lane & 7) * 16;
      DMA2(v2, r2, (unsigned)(size_t)(L + L_W2) + wave * 2048, 0u, 8192u);
      DMA2(vo, ro, (unsigned)(size_t)(L + L_WO) + wave * 4096, 0u, 65536u);
      DMA2(vo, ro, (unsigned)(size_t)(L + L_WO) + wave * 4096 + 2048, 1024u, 65536u + 1024u);
    }
    asm volatile("s_waitcnt vmcnt(%2)" : "+v"(qv[0]), "+v"(qv[1]) : "n"(32 - LK + 6));
    {
#define QBAD() (__builtin_amdgcn_ballot_w64(((sum4(qv[0]) ^ salt) | (sum4(qv[1]) ^ salt)) != 0) != 0)
      if (__builtin_expect(QBAD(), 0)) {
#pragma unroll 1
        for (unsigned spin = 0; spin < SPIN_CAP; ++spin) {
          FENCE();
          qv[0] = __builtin_amdgcn_raw_buffer_load_b128(rw, qsrc, 0, AUX_SC1); qv[1] = __builtin_amdgcn_raw_buffer_load_b128(rw, qsrc + 8192, 0, AUX_SC1);
          if (!QBAD()) break;
        }
      }
      const f32x4 q4 = halves_sum(vals(qv[0]) + vals(qv[1]));
      *(LAS u32x2*)(L + L_Q + wave * 256 + (lane & 31) * 8) = u32x2{cvtpk(q4[0], q4[1]), cvtpk(q4[2], q4[3])};
    }
    BAR();
    const int head = (r & 3) ^ ((r & 4) ? 3 : 0);
    const u32x4 qd = *(LAS u32x4*)(L + L_Q + head * 256 + g * 64 + (r >> 2) * 16);
    LAS unsigned char* sr = L + L_S + wave * 8192 + g * 128 + head * 32;
    LAS unsigned char* sw = sr + (r & 4) * 4 + (r >> 3) * 4;
    float m = -3.0e38f, y[4];
#define QP(B, P) do { const f32x4 z4 = {0.f, 0.f, 0.f, 0.f}, da = mfma16(tb[B][2 * (P)], qd, z4), db = mfma16(tb[B][2 * (P) + 1], qd, z4); \
    const float a0 = (r & 4) ? da[1] : da[0], a1 = (r & 4) ? da[3] : da[2], b0 = (r & 4) ? db[1] : db[0], b1 = (r & 4) ? db[3] : db[2]; \
    y[P] = ((r & 8) ? b1 : a0) + dpp<ROR8>((r & 8) ? a1 : b0); } while (0)
#define QE(T) do { const float z0 = ((r & 4) ? y[2] : y[0]) + dpp<HMIR>((r & 4) ? y[0] : y[2]), z1 = ((r & 4) ? y[3] : y[1]) + dpp<HMIR>((r & 4) ? y[1] : y[3]); \
    m = fmaxf(m, fmaxf(z0, z1)); *(LAS float*)(sw + (T) * 512) = z0; *(LAS float*)(sw + (T) * 512 + 8) = z1; } while (0)
#define QS(B, T) do { QP(B, 0); QP(B, 1); QP(B, 2); QP(B, 3); QE(T); SCHED(); } while (0)
#define CATCH(I) do { if ((I) < AH) LOAD(0, I, rk, koff, 4); } while (0)
    QS(0, 0); CATCH(0); CATCH(1); CATCH(2); CATCH(3); CATCH(4); CATCH(5); CATCH(6); CATCH(7);
    QS(1, 1); QS(2, 2); QS(3, 3);
    VMW(AH);
    *(volatile LAS int*)(L + L_PRG + wave * 4) = 1;
    LAS unsigned char* la = L + L_A + (wave * 4 + r) * A_STR + g * 16;
    PIN(la);
#define STEP(H, A, W, RS, VO, RN, VN, TN) do { VMW((W) - 1); \
    if ((H) + (A) < 16) LOAD(((H) + (A)) >> 3, ((H) + (A)) & 7, RS, VO, t + (((H) + (A)) >> 3)); else LOAD(0, ((H) + (A)) & 7, RN, VN, TN); } while (0)
#define STEP2(H, ...) do { STEP(H, __VA_ARGS__); STEP((H) + 1, __VA_ARGS__); } while (0)
#define KT(B, ...) do { STEP2(8 * (B), __VA_ARGS__); QP(B, 0); SCHED(); STEP2(8 * (B) + 2, __VA_ARGS__); QP(B, 1); SCHED(); STEP2(8 * (B) + 4, __VA_ARGS__); QP(B, 2); SCHED(); \
    STEP2(8 * (B) + 6, __VA_ARGS__); QP(B, 3); QE(t + (B)); SCHED(); } while (0)
#pragma unroll 1
    for (int t = 4; t < 16; t += 2) {
      const bool last = t == 14;
      const auto rn = __builtin_amdgcn_make_buffer_rsrc((last ? (char*)Vc : (char*)Kc) + kvo, 0, 2048 * 256, RS3);
      const unsigned vn = last ? voff : koff, tn = (t + 2) & 15;
      KT(0, AH, WK, rk, koff, rn, vn, tn); KT(1, AH, WK, rk, koff, rn, vn, tn);
    }
    m = fmaxf(m, dpp<HMIR>(m)); m = fmaxf(m, dpp<ROR8>(m));
    const float mc = xrow_max(m) * C2;
    float l = 0.f;
    f32x4 acc[8];
#pragma unroll
    for (int e = 0; e < 8; ++e) acc[e] = f32x4{0.f, 0.f, 0.f, 0.f};
    u32x4 pf, vt[8];
#define SP(T) do { const f32x4 s0 = *(LAS f32x4*)(sr + (T) * 512), s1 = *(LAS f32x4*)(sr + (T) * 512 + 16); float p[8]; \
    _Pragma("unroll") for (int i = 0; i < 4; ++i) { p[i] = __builtin_amdgcn_exp2f(fmaf(s0[i], C2, -mc)); p[4 + i] = __builtin_amdgcn_exp2f(fmaf(s1[i], C2, -mc)); } \
    l += ((p[0] + p[1]) + (p[2] + p[3])) + ((p[4] + p[5]) + (p[6] + p[7])); \
    pf = u32x4{cvtpk(p[0], p[1]), cvtpk(p[2], p[3]), cvtpk(p[4], p[5]), cvtpk(p[6], p[7])}; SCHED(); } while (0)
#define VP(B, P) do { _Pragma("unroll") for (int w = 0; w < 4; ++w) { vt[2 * w][P] = __builtin_amdgcn_perm(tb[B][2 * (P) + 1][w], tb[B][2 * (P)][w], 0x05040100u); \
      vt[2 * w + 1][P] = __builtin_amdgcn_perm(tb[B][2 * (P) + 1][w], tb[B][2 * (P)][w], 0x07060302u); } } while (0)
#define VE() do { _Pragma("unroll") for (int e = 0; e < 8; ++e) acc[e] = mfma16(vt[e], pf, acc[e]); SCHED(); } while (0)
#define VT(B, ...) do { STEP2(8 * (B), __VA_ARGS__); VP(B, 0); SP(t + (B)); STEP2(8 * (B) + 2, __VA_ARGS__); VP(B, 1); SCHED(); STEP2(8 * (B) + 4, __VA_ARGS__); VP(B, 2); SCHED(); \
    STEP2(8 * (B) + 6, __VA_ARGS__); VP(B, 3); VE(); } while (0)
#pragma unroll 1
    for (int t = 0; t < TE; t += 2) { VT(0, AH, WK, rv, voff, rv, voff, t + 2); VT(1, AH, WK, rv, voff, rv, voff, t + 2); }
#define REST(I) do { if ((I) >= AH) LOAD(0, I, rv, voff, TE); } while (0)
    REST(4); REST(5); REST(6); REST(7);
#pragma unroll 1
    for (int t = TE; t < 16; t += 2) { VT(0, 8, WE, rv, voff, rv, voff, t + 2); VT(1, 8, WE, rv, voff, rv, voff, t + 2); }
    l = xrow_sum(l);
    if (r < 4) {
#pragma unroll
      for (int e = 0; e < 8; ++e) *(LAS f32x4*)(la + e * 64) = acc[e];
      *(LAS float*)(L + L_M + r * 16 + wave * 4) = mc; *(LAS float*)(L + L_L + r * 16 + wave * 4) = l;
    }
    BAR();
  }
  asm volatile("" : "+s"(wave));
  if (wave >= 4) {
    __builtin_amdgcn_s_setprio(2);
    ARGS();
    const int n = wave - 4;
    gnext = *(const CAS u64*)(ws + WS_G) + 1;
    *(volatile LAS int*)(L + L_PRG + n * 4) = 0; *(volatile LAS int*)(L + L_F1 + n * 4) = 0; *(volatile LAS int*)(L + L_F1 + 16 + n * 4) = 0;
    unsigned qdst = r < 2 ? WS_QG + xcd * 16384 + (j & 3) * 4096 + ((j >> 2) * 4 + n) * 128 + r * 64 + g * 16 : OOB;
    unsigned hdst = (r < 2 && mlp) ? WS_HG + (j * 8 + xcd) * 512 + n * 128 + r * 64 + g * 16 : OOB;
    PIN(qdst); PIN(hdst);
    BAR();
    u32x4 xr = *(LAS u32x4*)(L + L_XN + n * 1024 + lane * 16);
#pragma unroll 1
    for (int it = n >> 1; it >= 0; --it) xr = ln8(xr);
    *(LAS u32x4*)(L + L_XN + n * 1024 + lane * 16) = xr;
    salt = SALT(gnext);
    asm volatile("s_waitcnt lgkmcnt(0)" : "+s"(salt), "+s"(gnext) :: "memory");
    *(volatile LAS int*)(L + L_F1 + 16 + n * 4) = 1;
#pragma unroll 1
    for (unsigned spin = 0; spin < 16 * SPIN_CAP; ++spin) {
      const u32x4 p = *(volatile LAS u32x4*)(L + L_F1) & *(volatile LAS u32x4*)(L + L_F1 + 16);
      if ((p[0] & p[1] & p[2] & p[3]) != 0) break;
    }
    FENCE();
    const int ao = (8 * g + (r >> 2)) * 128 + (r & 3) * 8 + n * 32;
    LAS unsigned char* const bp = L + L_XN + (r & 1) * 1024 + g * 16;
    f32x4 qv = {0.f, 0.f, 0.f, 0.f}, hv = {0.f, 0.f, 0.f, 0.f};
#pragma unroll
    for (int k = 0; k < 4; ++k) qv = mfma16(tr2(L + L_WQ + ao, k * 4096, k * 4096 + 512), *(LAS u32x4*)(bp + (j & 3) * 256 + k * 64), qv);
#pragma unroll
    for (int k = 0; k < 2; ++k) hv = mfma16(tr2(L + L_W1 + ao, k * 4096, k * 4096 + 512), *(LAS u32x4*)(bp + 2048 + xcd * 128 + k * 64), hv);
    __builtin_amdgcn_raw_buffer_store_b128(seal(qv, salt), rw, qdst, 0, AUX_SC1);
    __builtin_amdgcn_raw_buffer_store_b128(seal(hv, salt), rw, hdst, 0, AUX_SC1);
    asm volatile("s_barrier" ::: "memory");
  }
  asm volatile("" : "+s"(wave));
  if (wave == 6) {
    ARGS();
    __builtin_amdgcn_s_setprio(0);
#pragma unroll 1
    for (unsigned spin = 0; spin < SPIN_CAP; ++spin) {
      const u32x4 p = *(volatile LAS u32x4*)(L + L_PRG);
      if (min(min(p[0], p[1]), min(p[2], p[3])) != 0) break;
      __builtin_amdgcn_s_sleep(16);
    }
    SCHED();
    __builtin_amdgcn_s_setprio(3);
    const int hcol = ((lane >> 3) & 3) * 16 + (lane & 3) * 4;
    const u32x2 bb = *(const GAS u32x2*)(b1 + jm * 64 + hcol);
    const int ocol = xcd * 64 + (j & 15) * 4 + g;
    xres = blo(((const GAS u16*)x)[(lane & 1) * 512 + ocol]);
    bias2 = blo(((const GAS u16*)b2)[ocol]);
    u32x4 v[4];
#pragma unroll 1
    for (unsigned spin = 0; spin < SPIN_CAP; ++spin) {
      FENCE();
#pragma unroll
      for (int k = 0; k < 4; ++k) v[k] = __builtin_amdgcn_raw_buffer_load_b128(rw, WS_HG + jm * 4096 + k * 1024 + lane * 16, 0, AUX_SC1);
      if (__builtin_amdgcn_ballot_w64(((sum4(v[0]) ^ salt) | (sum4(v[1]) ^ salt) | (sum4(v[2]) ^ salt) | (sum4(v[3]) ^ salt)) != 0) == 0) break;
      __builtin_amdgcn_s_sleep(16);
    }
    SCHED();
    __builtin_amdgcn_s_setprio(0);
    const f32x4 hs = halves_sum((vals(v[0]) + vals(v[1])) + (vals(v[2]) + vals(v[3])));
    float hh[4];
#pragma unroll
    for (int i = 0; i < 4; ++i) {
      const float z = rbf(rbf(hs[i]) + ((i & 1) ? bhi(bb[i >> 1]) : blo(bb[i >> 1])));
      const float u = 0.7978845608028654f * (z + 0.044715f * z * z * z);
      hh[i] = z * __builtin_amdgcn_rcpf(1.f + __builtin_amdgcn_exp2f(-2.f * LOG2E * u));
    }
    *(LAS u32x2*)(L + L_H + ((lane >> 2) & 1) * 128 + hcol * 2) = u32x2{cvtpk(hh[0], hh[1]), cvtpk(hh[2], hh[3])};
    zp = seal(gemv64(L + L_W2, L + L_H + (r & 1) * 128, g, r), salt);
    PIN(zp); PIN(xres); PIN(bias2);
  }
  asm volatile("" : "+s"(wave));
  ARGS();
  if (wave >= 4) {
    unsigned dst1 = wave == 7 ? OOB : wave == 6 ? ((r < 8 && mlp) ? box + BX_ZB + (r >> 1) * 128 + (r & 1) * 64 + g * 16 : OOB)
                                                : WS_BOX + (akv * 32 + slot * 2 + ab) * BOX + BX_PA + ((wave & 1) * 64 + lane) * 16;
    const int hh = (wave & 1) * 2 + (lane >> 5);
    LAS unsigned char *lm = L + L_M + hh * 16, *la = L + L_A + hh * A_STR + (lane & 31) * 16;
    PIN(dst1); PIN(lm); PIN(la);
    asm volatile("" :: "s"(ws));
    BAR();
    if (wave < 6) {
      const f32x4 mm = *(LAS f32x4*)lm, ll = *(LAS f32x4*)(lm + (L_L - L_M));
      f32x4 a[4];
#pragma unroll
      for (int w = 0; w < 4; ++w) a[w] = *(LAS f32x4*)(la + w * 4 * A_STR);
      SCHED();
      const float M = max3(max3(mm[0], mm[1], mm[2]), mm[3], mm[3]);
      float wt[4];
#pragma unroll
      for (int w = 0; w < 4; ++w) wt[w] = __builtin_amdgcn_exp2f(mm[w] - M);
      const float Ls = fmaf(ll[3], wt[3], fmaf(ll[2], wt[2], fmaf(ll[1], wt[1], ll[0] * wt[0])));
      f32x4 o = a[0] * wt[0];
#pragma unroll
      for (int w = 1; w < 4; ++w) o = __builtin_elementwise_fma(a[w], f32x4{wt[w], wt[w], wt[w], wt[w]}, o);
      o *= __builtin_amdgcn_rcpf(Ls);
      const unsigned lse = __float_as_uint(M + __builtin_amdgcn_logf(Ls));
      const u32x4 w = __builtin_bit_cast(u32x4, o);
      const unsigned sw = sum4_top(w), so = (unsigned)__builtin_amdgcn_mov_dpp((int)sw, XOR1, 0xf, 0xf, true);
      zp = low_bytes(w, (lane & 1) ? lse : salt - sw - rotr(so + lse, 5));
    }
    __builtin_amdgcn_raw_buffer_store_b128(zp, rw, dst1, 0, AUX_SC1);
  }
  unsigned psrc = WS_BOX + (xcd * 32 + (lane >> 3) * 2 + (wave >> 2)) * BOX + BX_PA + (j >> 3) * 512 + (wave & 3) * 128 + (lane & 7) * 16;
  unsigned dst2 = r < 2 ? box + BX_SB + (wave & 3) * 128 + r * 64 + g * 16 : OOB;
  LAS unsigned char* lo = L + L_O + (wave >> 2) * 256 + ((wave & 3) * 32 + (lane & 7) * 4 + g) * 2;
  LAS unsigned char* lb = L + L_O + (r & 1) * 256 + g * 16;
  PIN(dst2); PIN(lo); PIN(lb);
  LAS unsigned char* const aw = L + L_WO + (8 * g + (r >> 2)) * 128 + (r & 3) * 8 + (wave & 3) * 32;
  u32x4 wa[4];
#pragma unroll
  for (int k = 0; k < 4; ++k) wa[k] = tr2(aw, k * 4096, k * 4096 + 512);
  {
    u32x4 v0, v1;
    int spin = SPIN_CAP;
    unsigned even = (lane & 1) - 1u;
    PIN(even);
    if (PD) __builtin_amdgcn_s_sleep(PD);
#pragma unroll 1
    do {
      FENCE();
      v0 = __builtin_amdgcn_raw_buffer_load_b128(rw, psrc, 0, AUX_SC1);
      v1 = __builtin_amdgcn_raw_buffer_load_b128(rw, psrc + 16 * BOX, 0, AUX_SC1);
      const unsigned s0 = sum4(v0), s1 = sum4(v1);
      const unsigned c0 = s0 + rotr((unsigned)__builtin_amdgcn_mov_dpp((int)s0, XOR1, 0xf, 0xf, true), 5), c1 = s1 + rotr((unsigned)__builtin_amdgcn_mov_dpp((int)s1, XOR1, 0xf, 0xf, true), 5);
      if (__builtin_amdgcn_ballot_w64((((c0 ^ salt) | (c1 ^ salt)) & even) != 0) == 0) break;
      if (PSL) __builtin_amdgcn_s_sleep(PSL);
      GIVE_UP(even);
    } while (true);
#define LOWB(v) __uint_as_float(__builtin_amdgcn_perm(__builtin_amdgcn_perm(v[1], v[2], 0x04000000u), __builtin_amdgcn_perm(v[3], v[0], 0x00000400u), 0x07060100u))
    const float e0 = dpp<ODD>(LOWB(v0)), e1 = dpp<ODD>(LOWB(v1));
    const float M = xrow_max(max3(max3(e0, e1, dpp<ROR8>(e0)), dpp<ROR8>(e1), e0));
    const float w0 = __builtin_amdgcn_exp2f(e0 - M), w1_ = __builtin_amdgcn_exp2f(e1 - M);
    float Ws = w0 + w1_; Ws += dpp<ROR8>(Ws); Ws = xrow_sum(Ws);
    float o = rows_reduce4(__builtin_elementwise_fma(vals(v1), f32x4{w1_, w1_, w1_, w1_}, vals(v0) * w0));
    o += dpp<ROR8>(o);
    o *= __builtin_amdgcn_rcpf(Ws);
    *(LAS u16*)lo = (u16)cvtpk(o, o);
  }
  BAR();
  if (wave < 4) {
    u32x4 ob[4];
#pragma unroll
    for (int k = 0; k < 4; ++k) ob[k] = *(LAS u32x4*)(lb + k * 64);
    SCHED();
    const f32x4 z4 = {0.f, 0.f, 0.f, 0.f};
    const f32x4 s = mfma16(wa[1], ob[1], mfma16(wa[0], ob[0], z4)) + mfma16(wa[3], ob[3], mfma16(wa[2], ob[2], z4));
    __builtin_amdgcn_raw_buffer_store_b128(seal(s, salt), rw, dst2, 0, AUX_SC1);
  }
  if (wave == 6 && j < 16) {
    const unsigned in = (j >> 2) * 128 + (lane & 1) * 64 + (j & 3) * 16;
    const unsigned zsrc = lane < 56 ? WS_BOX + (xcd * 32 + (lane >> 1)) * BOX + BX_ZB + in : OOB;
    const unsigned ssrc = WS_BOX + ((lane >> 3) * 32 + ((lane >> 1) & 3) * 8 + xcd) * BOX + BX_SB + in;
    u32x4 v;
    int spin = SPIN_CAP;
    unsigned need = lane < 56 ? ~0u : 0u;
#pragma unroll 1
    do {
      FENCE();
      v = __builtin_amdgcn_raw_buffer_load_b128(rw, zsrc, 0, AUX_SC1);
      if (__builtin_amdgcn_ballot_w64(((sum4(v) ^ salt) & need) != 0) == 0) break;
      GIVE_UP(need);
    } while (true);
    float z = rows_reduce4(vals(v));
    z += dpp<XOR2>(z); z += dpp<ROR4>(z); z += dpp<ROR8>(z);
    z = rbf(rbf(z) + bias2);
    float gate = rbf(z * __builtin_amdgcn_rcpf(1.f + __builtin_amdgcn_exp2f(-LOG2E * z)));
    unsigned odst = r < 2 ? (r * 512 + xcd * 64 + j * 4 + g) * 2 : OOB;
    PIN(gate); PIN(odst);
    spin = SPIN_CAP; need = ~0u;
#pragma unroll 1
    do {
      FENCE();
      v = __builtin_amdgcn_raw_buffer_load_b128(rw, ssrc, 0, AUX_SC1);
      if (__builtin_amdgcn_ballot_w64(((sum4(v) ^ salt) & need) != 0) == 0) break;
      GIVE_UP(need);
    } while (true);
    float s = rows_reduce4(vals(v));
    s += dpp<XOR2>(s); s += dpp<ROR4>(s); s += dpp<ROR8>(s);
    const float res = xres + rbf(rbf(s) * gate);
    const auto rout = __builtin_amdgcn_make_buffer_rsrc(out, 0, 2048, RS3);
    __builtin_amdgcn_raw_buffer_store_b16((u16)cvtpk(res, res), rout, odst, 0, 0);
    __builtin_amdgcn_raw_buffer_store_b64(u32x2{(unsigned)gnext, (unsigned)(gnext >> 32)}, rw, (blockIdx.x == 0 && lane == 0) ? WS_G : OOB, 0, AUX_SC1);
  }
}

extern "C" void kernel_launch(void* const* d_in, const int* in_sizes, int n_in, void* d_out, int out_size, void* d_ws, size_t ws_size, hipStream_t stream) {
  fused_decode<<<256, 512, 0, stream>>>((const u16*)d_in[0], (const u16*)d_in[1], (const u16*)d_in[2], (const u16*)d_in[3], (const u16*)d_in[4],
                                        (const u16*)d_in[6], (const u16*)d_in[7], (const u16*)d_in[8], (const u16*)d_in[9], (u16*)d_out, (unsigned char*)d_ws);
}
```

```cpp
#include <hip/hip_runtime.h>
#include <stdint.h>

typedef unsigned short u16;
typedef unsigned long long u64;
typedef unsigned u32x2 __attribute__((ext_vector_type(2)));
typedef unsigned u32x4 __attribute__((ext_vector_type(4)));
typedef float f32x2 __attribute__((ext_vector_type(2)));
typedef float f32x4 __attribute__((ext_vector_type(4)));
typedef short s16x4 __attribute__((ext_vector_type(4)));
typedef short bf16x8 __attribute__((ext_vector_type(8)));
typedef __bf16 bf16x2_t __attribute__((ext_vector_type(2)));
#define GAS __attribute__((address_space(1)))
#define LAS __attribute__((address_space(3)))
#define CAS __attribute__((address_space(4)))

#define WK 5
#define WE 8
#define TE 14
#define AH 6
#define PW 8
#define P0 1
#define LK 28
#define CB 112
#define CA 400
#define CD 0
#define CS 8
#define CL 2
#define BS 1
#define SD 0
#define TA 27
#define TS_ 0
#define SSL 0
#define ZE 0
#define PMN 0
#define BM 0xFFFF
#define PD 16
#define PSL 2
static_assert(WK + BS <= AH && AH <= 8 && WE <= 8 && TE % 2 == 0 && P0 < PW - 1 && PW <= 16 && LK >= 16 && LK <= 32, "");

constexpr int SK = 32768;
constexpr int AUX_SC1 = 16, AUX_NT = 2, RS3 = 0x00020000;
constexpr unsigned WS_G = 0, WS_QG = 4096, WS_HG = WS_QG + 8 * 16384, WS_TS = WS_HG + 224 * 512, WS_BOX = WS_TS + 16 * 512;
constexpr unsigned BOX = 3072 + 128, BX_PA = 0, BX_SB = 2048, BX_ZB = 2560, WS_END = WS_BOX + 256 * BOX;
constexpr int L_WQ = 0, L_WO = 16384, L_W1 = 32768, L_W2 = 40960, L_XN = 49152, L_Y = L_XN + 2048, L_Q = 53248, L_H = 54272, L_O = 54528,
              L_M = 55040, L_L = 55104, L_PRG = 55168, L_F1 = 55184, L_CK = 55216, L_LINE = 55224, L_A = 55296, A_STR = 528, L_S = L_A + 16 * A_STR, L_END = L_S + 32768;
constexpr unsigned OOB = 0x80000000u;
constexpr unsigned SPIN_CAP = 1u << 19;
constexpr float LOG2E = 1.4426950408889634f, C2 = 0.08838834764831845f * LOG2E;

#define SCHED() __builtin_amdgcn_sched_barrier(0)

__device__ __forceinline__ f32x4 mfma16(u32x4 a, u32x4 b, f32x4 c) {
  return __builtin_amdgcn_mfma_f32_16x16x32_bf16(__builtin_bit_cast(bf16x8, a), __builtin_bit_cast(bf16x8, b), c, 0, 0, 0);
}
__device__ __forceinline__ unsigned cvtpk(float lo, float hi) { return __builtin_bit_cast(unsigned, __builtin_convertvector(f32x2{lo, hi}, bf16x2_t)); }
__device__ __forceinline__ float blo(unsigned u) { return __uint_as_float(u << 16); }
__device__ __forceinline__ float bhi(unsigned u) { return __uint_as_float(u & 0xffff0000u); }
__device__ __forceinline__ float rbf(float x) { return blo(cvtpk(x, x)); }
template <int CTRL> __device__ __forceinline__ float dpp(float x) {
  return __builtin_bit_cast(float, __builtin_amdgcn_mov_dpp(__builtin_bit_cast(int, x), CTRL, 0xf, 0xf, true));
}
constexpr int XOR1 = 0xB1, XOR2 = 0x4E, ROR4 = 0x124, ROR8 = 0x128, HMIR = 0x141, ODD = 0xF5;
__device__ __forceinline__ float max3(float a, float b, float c) { float d; asm("v_max3_f32 %0, %1, %2, %3" : "=v"(d) : "v"(a), "v"(b), "v"(c)); return d; }
__device__ __forceinline__ float xrow_max(float x) {
  auto s = __builtin_amdgcn_permlane16_swap(__float_as_uint(x), __float_as_uint(x), false, false);
  x = max3(__uint_as_float(s[0]), __uint_as_float(s[1]), __uint_as_float(s[1]));
  auto t = __builtin_amdgcn_permlane32_swap(__float_as_uint(x), __float_as_uint(x), false, false);
  return max3(__uint_as_float(t[0]), __uint_as_float(t[1]), __uint_as_float(t[1]));
}
__device__ __forceinline__ float xrow_sum(float x) {
  auto s = __builtin_amdgcn_permlane16_swap(__float_as_uint(x), __float_as_uint(x), false, false);
  x = __uint_as_float(s[0]) + __uint_as_float(s[1]);
  auto t = __builtin_amdgcn_permlane32_swap(__float_as_uint(x), __float_as_uint(x), false, false);
  return __uint_as_float(t[0]) + __uint_as_float(t[1]);
}
__device__ __forceinline__ float wave_sum(float x) {
  x += dpp<XOR1>(x); x += dpp<XOR2>(x); x += dpp<ROR4>(x); x += dpp<ROR8>(x); return xrow_sum(x);
}
__device__ __forceinline__ float rows_reduce4(f32x4 v) {
  auto a = __builtin_amdgcn_permlane32_swap(__float_as_uint(v[0]), __float_as_uint(v[2]), false, false);
  auto b = __builtin_amdgcn_permlane32_swap(__float_as_uint(v[1]), __float_as_uint(v[3]), false, false);
  const float u0 = __uint_as_float(a[0]) + __uint_as_float(a[1]), u1 = __uint_as_float(b[0]) + __uint_as_float(b[1]);
  auto c = __builtin_amdgcn_permlane16_swap(__float_as_uint(u0), __float_as_uint(u1), false, false);
  return __uint_as_float(c[0]) + __uint_as_float(c[1]);
}
__device__ __forceinline__ u32x4 ln8(u32x4 v) {
  float f[8];
#pragma unroll
  for (int i = 0; i < 4; ++i) { f[2 * i] = blo(v[i]); f[2 * i + 1] = bhi(v[i]); }
  const float mean = wave_sum(((f[0] + f[1]) + (f[2] + f[3])) + ((f[4] + f[5]) + (f[6] + f[7]))) * (1.f / 512.f);
  float ss = 0.f;
#pragma unroll
  for (int i = 0; i < 8; ++i) { const float d = f[i] - mean; ss = fmaf(d, d, ss); }
  const float var = wave_sum(ss) * (1.f / 512.f);
  const float mb = rbf(mean), inv = __builtin_amdgcn_rcpf(rbf(__builtin_amdgcn_sqrtf(rbf(rbf(var) + 1e-5f))));
  u32x4 o;
#pragma unroll
  for (int i = 0; i < 4; ++i) { const unsigned c = cvtpk(f[2 * i] - mb, f[2 * i + 1] - mb); o[i] = cvtpk(blo(c) * inv, bhi(c) * inv); }
  return o;
}
__device__ __forceinline__ u32x4 tr2(LAS unsigned char* p, int o0, int o1) {
  const u32x2 a = __builtin_bit_cast(u32x2, __builtin_amdgcn_ds_read_tr16_b64_v4i16((LAS s16x4*)(p + o0)));
  const u32x2 b = __builtin_bit_cast(u32x2, __builtin_amdgcn_ds_read_tr16_b64_v4i16((LAS s16x4*)(p + o1)));
  return u32x4{a[0], a[1], b[0], b[1]};
}
__device__ __forceinline__ f32x4 gemv64(LAS unsigned char* img, LAS unsigned char* bp, int g, int r) {
  LAS unsigned char* const a = img + (8 * g + (r >> 2)) * 128 + (r & 3) * 8;
  const u32x4 b0 = *(LAS u32x4*)(bp + g * 16), b1 = *(LAS u32x4*)(bp + 64 + g * 16);
  f32x4 z[4];
#pragma unroll
  for (int n = 0; n < 4; ++n) {
    z[n] = mfma16(tr2(a, n * 32, n * 32 + 512), b0, f32x4{0.f, 0.f, 0.f, 0.f});
    z[n] = mfma16(tr2(a, 4096 + n * 32, 4096 + n * 32 + 512), b1, z[n]);
  }
  return (r & 4) ? ((r & 2) ? z[3] : z[2]) : ((r & 2) ? z[1] : z[0]);
}
#define BAR() asm volatile("s_waitcnt lgkmcnt(0)\n\ts_barrier" ::: "memory")
#define FENCE() asm volatile("" ::: "memory")
#define VMW(N) do { __builtin_amdgcn_s_waitcnt(0x0F70 | ((N) & 15) | (((N) >> 4) << 14)); SCHED(); } while (0)

__device__ __forceinline__ unsigned rotr(unsigned x, unsigned n) { return __builtin_amdgcn_alignbit(x, x, n); }
__device__ __forceinline__ unsigned sum4(u32x4 w) { return w[0] + rotr(w[1], 8) + rotr(w[2], 16) + rotr(w[3], 24); }
__device__ __forceinline__ unsigned sum4_top(u32x4 w) {
  return (w[0] & 0xffffff00u) + __builtin_amdgcn_perm(0u, w[1], 0x0c030201u) + __builtin_amdgcn_perm(0u, w[2], 0x010c0302u) + __builtin_amdgcn_perm(0u, w[3], 0x02010c03u);
}
__device__ __forceinline__ u32x4 low_bytes(u32x4 w, unsigned d) {
  return u32x4{__builtin_amdgcn_perm(w[0], d, 0x07060500u), __builtin_amdgcn_perm(w[1], d, 0x07060503u), __builtin_amdgcn_perm(w[2], d, 0x07060502u), __builtin_amdgcn_perm(w[3], d, 0x07060501u)};
}
__device__ __forceinline__ u32x4 seal(f32x4 v, unsigned salt) { const u32x4 w = __builtin_bit_cast(u32x4, v); return low_bytes(w, salt - sum4_top(w)); }
__device__ __forceinline__ f32x4 vals(u32x4 w) { return __builtin_bit_cast(f32x4, w & 0xffffff00u); }
__device__ __forceinline__ f32x4 halves_sum(f32x4 v) {
  auto a = __builtin_amdgcn_permlane32_swap(__float_as_uint(v[0]), __float_as_uint(v[0]), false, false), b = __builtin_amdgcn_permlane32_swap(__float_as_uint(v[1]), __float_as_uint(v[1]), false, false);
  auto c = __builtin_amdgcn_permlane32_swap(__float_as_uint(v[2]), __float_as_uint(v[2]), false, false), d = __builtin_amdgcn_permlane32_swap(__float_as_uint(v[3]), __float_as_uint(v[3]), false, false);
  return f32x4{__uint_as_float(a[0]) + __uint_as_float(a[1]), __uint_as_float(b[0]) + __uint_as_float(b[1]), __uint_as_float(c[0]) + __uint_as_float(c[1]), __uint_as_float(d[0]) + __uint_as_float(d[1])};
}
#define SALT(g) ((unsigned)(g) * 0x9E3779B1u + 0x7F4A7C15u)
#define GIVE_UP(mask) do { --spin; mask = spin > 0 ? mask : 0u; asm volatile("" : "+v"(mask), "+v"(spin)); } while (0)
#define PIN(x) asm volatile("" : "+v"(x))

__global__ void __launch_bounds__(512, 2)
fused_decode(const u16* a_x, const u16* a_k, const u16* a_v, const u16* a_wq, const u16* a_wo, const u16* a_w1, const u16* a_b1, const u16* a_w2, const u16* a_b2,
             u16* a_out, unsigned char* a_ws) {
  __shared__ __attribute__((aligned(16))) unsigned char smem[L_END];
  LAS unsigned char* const L = (LAS unsigned char*)smem;
#define ARGS() const CAS u64* kp_ = (const CAS u64*)__builtin_amdgcn_kernarg_segment_ptr(); asm volatile("" : "+s"(kp_)); \
  const u16* const x = (const u16*)kp_[0]; const u16* const Kc = (const u16*)kp_[1]; const u16* const Vc = (const u16*)kp_[2]; const u16* const wq = (const u16*)kp_[3]; \
  const u16* const wo = (const u16*)kp_[4]; const u16* const w1 = (const u16*)kp_[5]; const u16* const b1 = (const u16*)kp_[6]; const u16* const w2 = (const u16*)kp_[7]; \
  const u16* const b2 = (const u16*)kp_[8]; u16* const out = (u16*)kp_[9]; unsigned char* const ws = (unsigned char*)kp_[10]; \
  const auto rw = __builtin_amdgcn_make_buffer_rsrc(ws, 0, WS_END, RS3); \
  (void)x; (void)Kc; (void)Vc; (void)wq; (void)wo; (void)w1; (void)b1; (void)w2; (void)b2; (void)out; (void)rw
  const int lane = threadIdx.x & 63;
  int wave = __builtin_amdgcn_readfirstlane(threadIdx.x >> 6);
  const int xcd = blockIdx.x & 7, j = blockIdx.x >> 3, lw = xcd * 32 + j;
  const int g = lane >> 4, r = lane & 15;
  const unsigned box = WS_BOX + lw * BOX;
  const int pr = (xcd & 3) * 32 + j, ab = (pr >> 3) & 1, akv = pr >> 4, slot = (pr & 7) * 2 + (xcd >> 2);
  const bool mlp = j < 28;
  const int jm = mlp ? j : 0;
  float xres = 0.f, bias2 = 0.f;
  u32x4 zp = {0u, 0u, 0u, 0u};
  unsigned salt = 0;
  u64 gnext = 0;

  if (wave < 4) {
    unsigned line = (unsigned)__builtin_amdgcn_s_memrealtime() + CA;
    ARGS();
    asm volatile("s_setprio 3" ::: "memory");
    SCHED();
    unsigned vq = (wave * 32 + (lane >> 3)) * 8192 + (lane & 7) * 16, v1 = (wave * 16 + (lane >> 3)) * 3584 + (lane & 7) * 16;
    unsigned vx = (wave & 1) * 1024 + lane * 16;
    unsigned qo = ((j & 3) * 128 * 4096 + xcd * 512 + (j >> 2) * 64) * 2, o1 = (xcd * 64 * 1792 + jm * 64) * 2, len1 = mlp ? 63u * 3584u + 128u : 0u;
    constexpr unsigned PSTR = 8192, WSTR = 2048;
    unsigned koff = wave * WSTR + (r >> 2) * 256 + g * 64 + (r & 3) * 16, voff = wave * WSTR + lane * 16, kvo = ((ab * 8 + akv) * SK + slot * 2048) * 256u;
    asm volatile("" : "+v"(vx), "+v"(vq), "+v"(v1), "+v"(koff), "+v"(voff), "+s"(qo), "+s"(o1), "+s"(len1), "+s"(kvo));
    {
      const u64 qb = (u64)wq + qo, b1_ = (u64)w1 + o1;
      const u32x4 rq = {(unsigned)qb, (unsigned)(qb >> 32) & 0xffffu, 127u * 8192u + 128u, (unsigned)RS3};
      const u32x4 r1 = {(unsigned)b1_, (unsigned)(b1_ >> 32) & 0xffffu, len1, (unsigned)RS3};
      const u32x4 rx = {(unsigned)(u64)x, (unsigned)((u64)x >> 32) & 0xffffu, 2048u, (unsigned)RS3};
      asm volatile("s_mov_b32 m0, %12\n\ts_nop 3\n\tbuffer_load_dwordx4 %10, %11, 0 offen lds\n\t"
                   "s_mov_b32 m0, %2\n\ts_nop 0\n\t"
                   "buffer_load_dwordx4 %0, %1, 0 offen nt lds\n\t"
                   "s_add_u32 m0, m0, 0x400\n\ts_nop 0\n\tbuffer_load_dwordx4 %0, %1, %3 offen nt lds\n\t"
                   "s_add_u32 m0, m0, 0x400\n\ts_nop 0\n\tbuffer_load_dwordx4 %0, %1, %4 offen nt lds\n\t"
                   "s_add_u32 m0, m0, 0x400\n\ts_nop 0\n\tbuffer_load_dwordx4 %0, %1, %5 offen nt lds\n\t"
                   "s_mov_b32 m0, %8\n\ts_nop 0\n\tbuffer_load_dwordx4 %6, %7, 0 offen nt lds\n\t"
                   "s_add_u32 m0, m0, 0x400\n\ts_nop 0\n\tbuffer_load_dwordx4 %6, %7, %9 offen nt lds"
                   :: "v"(vq), "s"(rq), "s"((unsigned)(size_t)(L + L_WQ) + wave * 4096), "s"(0x10000u), "s"(0x20000u), "s"(0x30000u),
                      "v"(v1), "s"(r1), "s"((unsigned)(size_t)(L + L_W1) + wave * 2048), "s"(8u * 3584u),
                      "v"(vx), "s"(rx), "s"((unsigned)(size_t)(L + L_XN) + wave * 1024) : "memory", "scc");
    }
    __builtin_amdgcn_s_setprio(0);
    const CAS u64* gp = (const CAS u64*)(ws + WS_G);
    asm volatile("" : "+s"(gp));
    const u64 gprev = *gp;
    const auto rk = __builtin_amdgcn_make_buffer_rsrc((char*)Kc + kvo, 0, 2048 * 256, RS3);
    const auto rv = __builtin_amdgcn_make_buffer_rsrc((char*)Vc + kvo, 0, 2048 * 256, RS3);
    u32x4 tb[4][8];
#define LOAD(B, I, RS, VO, T) do { tb[B][I] = __builtin_amdgcn_raw_buffer_load_b128(RS, (VO) + ((I) & 1) * 1024, (unsigned)(T) * 32768u + ((I) >> 1) * PSTR, AUX_NT); SCHED(); } while (0)
#define PRE(H) do { if ((H) == P0) asm volatile("s_waitcnt vmcnt(%0)\n\ts_barrier" :: "n"(6 + P0) : "memory");   \
    if ((H) > P0) VMW(PW - 1); \
    if ((H) == PW - 1) { FENCE(); *(volatile LAS int*)(L + L_F1 + wave * 4) = 1; SCHED(); }     \
    LOAD((H) >> 3, (H) & 7, rk, koff, (H) >> 3); } while (0)
#define PRE2(H) do { if ((H) < LK) PRE(H); } while (0)
#define PRE3(H) do { if ((H) >= LK) PRE(H); } while (0)
#define X8(M, H) do { M(H); M((H) + 1); M((H) + 2); M((H) + 3); M((H) + 4); M((H) + 5); M((H) + 6); M((H) + 7); } while (0)
    SCHED();
    X8(PRE, 0); X8(PRE, 8);
    { u64 g_ = gprev; asm volatile("" : "+s"(g_)); salt = SALT(g_ + 1); }
    X8(PRE2, 16); X8(PRE2, 24);
    const unsigned qsrc = WS_QG + akv * 16384 + (lane >> 5) * 4096 + (wave * 8 + ((lane >> 2) & 7)) * 128 + ab * 64 + (lane & 3) * 16;
    u32x4 qv[2];
    {
      const u32x4 rwq = {(unsigned)(u64)ws, (unsigned)((u64)ws >> 32) & 0xffffu, WS_END, (unsigned)RS3};
      asm volatile("s_waitcnt vmcnt(%5)\n\tbuffer_load_dwordx4 %0, %2, %3, 0 offen sc1\n\tbuffer_load_dwordx4 %1, %2, %3, %4 offen sc1"
                   : "=&v"(qv[0]), "=&v"(qv[1]) : "v"(qsrc), "s"(rwq), "s"(8192u), "n"(PW - 2) : "memory");
    }
    X8(PRE3, 16); X8(PRE3, 24);
    {
      const u64 b2_ = (u64)(w2 + jm * 64 * 512 + xcd * 64), bo = (u64)(wo + (xcd * 512 + (j >> 3) * 128) * 512 + (j & 7) * 64);
      const u32x4 r2 = {(unsigned)b2_, (unsigned)(b2_ >> 32) & 0xffffu, mlp ? 63u * 1024u + 128u : 0u, (unsigned)RS3};
      const u32x4 ro = {(unsigned)bo, (unsigned)(bo >> 32) & 0xffffu, 127u * 1024u + 128u, (unsigned)RS3};
#define DMA2(V, R, LB, S0, S1) asm volatile("s_waitcnt vmcnt(%5)\n\ts_mov_b32 m0, %2\n\ts_nop 0\n\tbuffer_load_dwordx4 %0, %1, %3 offen nt lds\n\t" \
                     "s_add_u32 m0, m0, 0x400\n\ts_waitcnt vmcnt(%5)\n\tbuffer_load_dwordx4 %0, %1, %4 offen nt lds" :: "v"(V), "s"(R), "s"(LB), "s"(S0), "s"(S1), "n"(PW - 1) : "memory", "scc")
      const unsigned v2 = (wave * 16 + (lane >> 3)) * 1024 + (lane & 7) * 16, vo = (lane >> 3) * 8192 + wave * 2048 + (lane & 7) * 16;
      DMA2(v2, r2, (unsigned)(size_t)(L + L_W2) + wave * 2048, 0u, 8192u);
      DMA2(vo, ro, (unsigned)(size_t)(L + L_WO) + wave * 4096, 0u, 65536u);
      DMA2(vo, ro, (unsigned)(size_t)(L + L_WO) + wave * 4096 + 2048, 1024u, 65536u + 1024u);
    }
    asm volatile("s_waitcnt vmcnt(%2)" : "+v"(qv[0]), "+v"(qv[1]) : "n"(32 - LK + 6));
    {
#define QBAD() (__builtin_amdgcn_ballot_w64(((sum4(qv[0]) ^ salt) | (sum4(qv[1]) ^ salt)) != 0) != 0)
      if (__builtin_expect(QBAD(), 0)) {
#pragma unroll 1
        for (unsigned spin = 0; spin < SPIN_CAP; ++spin) {
          FENCE();
          qv[0] = __builtin_amdgcn_raw_buffer_load_b128(rw, qsrc, 0, AUX_SC1); qv[1] = __builtin_amdgcn_raw_buffer_load_b128(rw, qsrc + 8192, 0, AUX_SC1);
          if (!QBAD()) break;
        }
      }
      const f32x4 q4 = halves_sum(vals(qv[0]) + vals(qv[1]));
      *(LAS u32x2*)(L + L_Q + wave * 256 + (lane & 31) * 8) = u32x2{cvtpk(q4[0], q4[1]), cvtpk(q4[2], q4[3])};
    }
    BAR();
    const int head = (r & 3) ^ ((r & 4) ? 3 : 0);
    const u32x4 qd = *(LAS u32x4*)(L + L_Q + head * 256 + g * 64 + (r >> 2) * 16);
    LAS unsigned char* sr = L + L_S + wave * 8192 + g * 128 + head * 32;
    LAS unsigned char* sw = sr + (r & 4) * 4 + (r >> 3) * 4;
    float m = -3.0e38f, y[4];
#define QP(B, P) do { const f32x4 z4 = {0.f, 0.f, 0.f, 0.f}, da = mfma16(tb[B][2 * (P)], qd, z4), db = mfma16(tb[B][2 * (P) + 1], qd, z4); \
    const float a0 = (r & 4) ? da[1] : da[0], a1 = (r & 4) ? da[3] : da[2], b0 = (r & 4) ? db[1] : db[0], b1 = (r & 4) ? db[3] : db[2]; \
    y[P] = ((r & 8) ? b1 : a0) + dpp<ROR8>((r & 8) ? a1 : b0); } while (0)
#define QE(T) do { const float z0 = ((r & 4) ? y[2] : y[0]) + dpp<HMIR>((r & 4) ? y[0] : y[2]), z1 = ((r & 4) ? y[3] : y[1]) + dpp<HMIR>((r & 4) ? y[1] : y[3]); \
    m = fmaxf(m, fmaxf(z0, z1)); *(LAS float*)(sw + (T) * 512) = z0; *(LAS float*)(sw + (T) * 512 + 8) = z1; } while (0)
#define QS(B, T) do { QP(B, 0); QP(B, 1); QP(B, 2); QP(B, 3); QE(T); SCHED(); } while (0)
#define CATCH(I) do { if ((I) < AH) LOAD(0, I, rk, koff, 4); } while (0)
    QS(0, 0); CATCH(0); CATCH(1); CATCH(2); CATCH(3); CATCH(4); CATCH(5); CATCH(6); CATCH(7);
    QS(1, 1); QS(2, 2); QS(3, 3);
    VMW(AH);
    *(volatile LAS int*)(L + L_PRG + wave * 4) = 1;
    LAS unsigned char* la = L + L_A + (wave * 4 + r) * A_STR + g * 16;
    PIN(la);
    int lag = 0;
#define DYNW(H, W) do { if ((W) == WK && !((PMN >> (H)) & 1) && !((BM >> (H)) & 1)) VMW(WK - 1); else if ((W) == WK) asm volatile("s_cmp_gt_i32 %0, 0\n\ts_cbranch_scc1 .Lw%=\n\ts_waitcnt vmcnt(%1)\n.Lw%=:\n\ts_waitcnt vmcnt(%2)" :: "s"(lag), "n"(WK - 1 - ((PMN >> (H)) & 1)), "n"(WK - 1 + BS * ((BM >> (H)) & 1)) : "scc"); else VMW((W) - 1); SCHED(); } while (0)
#define CTL0(W) const unsigned now_ = (W) == WK ? (unsigned)__builtin_amdgcn_s_memrealtime() : 0u; const u32x2 lv_ = (W) == WK ? *(volatile LAS u32x2*)(L + L_LINE) : u32x2{0u, 0u}; SCHED()
#define CTL1(W, N) do { if ((W) == WK) { line = __builtin_amdgcn_readfirstlane(lv_[0] ? lv_[1] : line); lag = (int)(now_ - line - (N) * CB); \
      if (wave == 0) *(volatile LAS u32x2*)(L + L_CK) = u32x2{(unsigned)(N), now_}; } SCHED(); } while (0)
#define STEP(H, A, W, RS, VO, RN, VN, TN) do { DYNW(H, W); \
    if ((H) + (A) < 16) LOAD(((H) + (A)) >> 3, ((H) + (A)) & 7, RS, VO, t + (((H) + (A)) >> 3)); else LOAD(0, ((H) + (A)) & 7, RN, VN, TN); } while (0)
#define STEP2(H, ...) do { STEP(H, __VA_ARGS__); STEP((H) + 1, __VA_ARGS__); } while (0)
#define KT(B, A, W, ...) do { CTL0(W); STEP2(8 * (B), A, W, __VA_ARGS__); CTL1(W, t + (B)); QP(B, 0); SCHED(); STEP2(8 * (B) + 2, A, W, __VA_ARGS__); QP(B, 1); SCHED(); STEP2(8 * (B) + 4, A, W, __VA_ARGS__); QP(B, 2); SCHED(); \
    STEP2(8 * (B) + 6, A, W, __VA_ARGS__); QP(B, 3); QE(t + (B)); SCHED(); } while (0)
#pragma unroll 1
    for (int t = 4; t < 16; t += 2) {
      const bool last = t == 14;
      const auto rn = __builtin_amdgcn_make_buffer_rsrc((last ? (char*)Vc : (char*)Kc) + kvo, 0, 2048 * 256, RS3);
      const unsigned vn = last ? voff : koff, tn = (t + 2) & 15;
      KT(0, AH, WK, rk, koff, rn, vn, tn); KT(1, AH, WK, rk, koff, rn, vn, tn);
    }
    m = fmaxf(m, dpp<HMIR>(m)); m = fmaxf(m, dpp<ROR8>(m));
    const float mc = xrow_max(m) * C2;
    float l = 0.f;
    f32x4 acc[8];
#pragma unroll
    for (int e = 0; e < 8; ++e) acc[e] = f32x4{0.f, 0.f, 0.f, 0.f};
    u32x4 pf, vt[8];
#define SP(T) do { const f32x4 s0 = *(LAS f32x4*)(sr + (T) * 512), s1 = *(LAS f32x4*)(sr + (T) * 512 + 16); float p[8]; \
    _Pragma("unroll") for (int i = 0; i < 4; ++i) { p[i] = __builtin_amdgcn_exp2f(fmaf(s0[i], C2, -mc)); p[4 + i] = __builtin_amdgcn_exp2f(fmaf(s1[i], C2, -mc)); } \
    l += ((p[0] + p[1]) + (p[2] + p[3])) + ((p[4] + p[5]) + (p[6] + p[7])); \
    pf = u32x4{cvtpk(p[0], p[1]), cvtpk(p[2], p[3]), cvtpk(p[4], p[5]), cvtpk(p[6], p[7])}; SCHED(); } while (0)
#define VP(B, P) do { _Pragma("unroll") for (int w = 0; w < 4; ++w) { vt[2 * w][P] = __builtin_amdgcn_perm(tb[B][2 * (P) + 1][w], tb[B][2 * (P)][w], 0x05040100u); \
      vt[2 * w + 1][P] = __builtin_amdgcn_perm(tb[B][2 * (P) + 1][w], tb[B][2 * (P)][w], 0x07060302u); } } while (0)
#define VE() do { _Pragma("unroll") for (int e = 0; e < 8; ++e) acc[e] = mfma16(vt[e], pf, acc[e]); SCHED(); } while (0)
#define VT(B, A, W, ...) do { CTL0(W); STEP2(8 * (B), A, W, __VA_ARGS__); CTL1(W, 16 + t + (B)); VP(B, 0); SP(t + (B)); STEP2(8 * (B) + 2, A, W, __VA_ARGS__); VP(B, 1); SCHED(); STEP2(8 * (B) + 4, A, W, __VA_ARGS__); VP(B, 2); SCHED(); \
    STEP2(8 * (B) + 6, A, W, __VA_ARGS__); VP(B, 3); VE(); } while (0)
#pragma unroll 1
    for (int t = 0; t < TE; t += 2) { VT(0, AH, WK, rv, voff, rv, voff, t + 2); VT(1, AH, WK, rv, voff, rv, voff, t + 2); }
#define REST(I) do { if ((I) >= AH) LOAD(0, I, rv, voff, TE); } while (0)
    REST(4); REST(5); REST(6); REST(7);
#pragma unroll 1
    for (int t = TE; t < 16; t += 2) { VT(0, 8, WE, rv, voff, rv, voff, t + 2); VT(1, 8, WE, rv, voff, rv, voff, t + 2); }
    l = xrow_sum(l);
    if (r < 4) {
#pragma unroll
      for (int e = 0; e < 8; ++e) *(LAS f32x4*)(la + e * 64) = acc[e];
      *(LAS float*)(L + L_M + r * 16 + wave * 4) = mc; *(LAS float*)(L + L_L + r * 16 + wave * 4) = l;
    }
    BAR();
  }
  asm volatile("" : "+s"(wave));
  if (wave >= 4) {
    __builtin_amdgcn_s_setprio(2);
    ARGS();
    const int n = wave - 4;
    gnext = *(const CAS u64*)(ws + WS_G) + 1;
    *(volatile LAS int*)(L + L_PRG + n * 4) = 0; *(volatile LAS int*)(L + L_F1 + n * 4) = 0; *(volatile LAS int*)(L + L_F1 + 16 + n * 4) = 0; *(volatile LAS int*)(L + L_CK + n * 4) = 0;
    unsigned qdst = r < 2 ? WS_QG + xcd * 16384 + (j & 3) * 4096 + ((j >> 2) * 4 + n) * 128 + r * 64 + g * 16 : OOB;
    unsigned hdst = (r < 2 && mlp) ? WS_HG + (j * 8 + xcd) * 512 + n * 128 + r * 64 + g * 16 : OOB;
    PIN(qdst); PIN(hdst);
    BAR();
    u32x4 xr = *(LAS u32x4*)(L + L_XN + n * 1024 + lane * 16);
#pragma unroll 1
    for (int it = n >> 1; it >= 0; --it) xr = ln8(xr);
    *(LAS u32x4*)(L + L_XN + n * 1024 + lane * 16) = xr;
    salt = SALT(gnext);
    asm volatile("s_waitcnt lgkmcnt(0)" : "+s"(salt), "+s"(gnext) :: "memory");
    *(volatile LAS int*)(L + L_F1 + 16 + n * 4) = 1;
#pragma unroll 1
    for (unsigned spin = 0; spin < 16 * SPIN_CAP; ++spin) {
      const u32x4 p = *(volatile LAS u32x4*)(L + L_F1) & *(volatile LAS u32x4*)(L + L_F1 + 16);
      if ((p[0] & p[1] & p[2] & p[3]) != 0) break;
    }
    FENCE();
    const int ao = (8 * g + (r >> 2)) * 128 + (r & 3) * 8 + n * 32;
    LAS unsigned char* const bp = L + L_XN + (r & 1) * 1024 + g * 16;
    f32x4 qv = {0.f, 0.f, 0.f, 0.f}, hv = {0.f, 0.f, 0.f, 0.f};
#pragma unroll
    for (int k = 0; k < 4; ++k) qv = mfma16(tr2(L + L_WQ + ao, k * 4096, k * 4096 + 512), *(LAS u32x4*)(bp + (j & 3) * 256 + k * 64), qv);
#pragma unroll
    for (int k = 0; k < 2; ++k) hv = mfma16(tr2(L + L_W1 + ao, k * 4096, k * 4096 + 512), *(LAS u32x4*)(bp + 2048 + xcd * 128 + k * 64), hv);
    __builtin_amdgcn_raw_buffer_store_b128(seal(qv, salt), rw, qdst, 0, AUX_SC1);
    __builtin_amdgcn_raw_buffer_store_b128(seal(hv, salt), rw, hdst, 0, AUX_SC1);
    asm volatile("s_barrier" ::: "memory");
  }
  asm volatile("" : "+s"(wave));
  if (wave == 6) {
    ARGS();
    __builtin_amdgcn_s_setprio(0);
#pragma unroll 1
    for (unsigned spin = 0; spin < SPIN_CAP; ++spin) {
      const u32x4 p = *(volatile LAS u32x4*)(L + L_PRG);
      if (min(min(p[0], p[1]), min(p[2], p[3])) != 0) break;
      __builtin_amdgcn_s_sleep(16);
    }
    SCHED();
    __builtin_amdgcn_s_setprio(3);
    const int hcol = ((lane >> 3) & 3) * 16 + (lane & 3) * 4;
    const u32x2 bb = *(const GAS u32x2*)(b1 + jm * 64 + hcol);
    const int ocol = xcd * 64 + (j & 15) * 4 + g;
    xres = blo(((const GAS u16*)x)[(lane & 1) * 512 + ocol]);
    bias2 = blo(((const GAS u16*)b2)[ocol]);
    u32x4 v[4];
#pragma unroll 1
    for (unsigned spin = 0; spin < SPIN_CAP; ++spin) {
      FENCE();
#pragma unroll
      for (int k = 0; k < 4; ++k) v[k] = __builtin_amdgcn_raw_buffer_load_b128(rw, WS_HG + jm * 4096 + k * 1024 + lane * 16, 0, AUX_SC1);
      if (__builtin_amdgcn_ballot_w64(((sum4(v[0]) ^ salt) | (sum4(v[1]) ^ salt) | (sum4(v[2]) ^ salt) | (sum4(v[3]) ^ salt)) != 0) == 0) break;
      __builtin_amdgcn_s_sleep(16);
    }
    SCHED();
    __builtin_amdgcn_s_setprio(0);
    const f32x4 hs = halves_sum((vals(v[0]) + vals(v[1])) + (vals(v[2]) + vals(v[3])));
    float hh[4];
#pragma unroll
    for (int i = 0; i < 4; ++i) {
      const float z = rbf(rbf(hs[i]) + ((i & 1) ? bhi(bb[i >> 1]) : blo(bb[i >> 1])));
      const float u = 0.7978845608028654f * (z + 0.044715f * z * z * z);
      hh[i] = z * __builtin_amdgcn_rcpf(1.f + __builtin_amdgcn_exp2f(-2.f * LOG2E * u));
    }
    *(LAS u32x2*)(L + L_H + ((lane >> 2) & 1) * 128 + hcol * 2) = u32x2{cvtpk(hh[0], hh[1]), cvtpk(hh[2], hh[3])};
    zp = seal(gemv64(L + L_W2, L + L_H + (r & 1) * 128, g, r), salt);
    if (ZE) __builtin_amdgcn_raw_buffer_store_b128(zp, rw, (r < 8 && mlp) ? box + BX_ZB + (r >> 1) * 128 + (r & 1) * 64 + g * 16 : OOB, 0, AUX_SC1);
    PIN(zp); PIN(xres); PIN(bias2);
  }
  asm volatile("" : "+s"(wave));
  ARGS();
  if (wave == 7) {
    const bool sample = (j & 7) == ((xcd + 3 * (j >> 3)) & 7);
#pragma unroll 1
    for (int n = 4, k = 0; n + CL < 16 + TE; n += CS, ++k) {
      u32x2 ck;
#pragma unroll 1
      for (unsigned spin = 0; spin < SPIN_CAP; ++spin) { ck = *(volatile LAS u32x2*)(L + L_CK); if ((int)ck[0] >= n) break; __builtin_amdgcn_s_sleep(4); }
      const unsigned tk = ck[1] - (ck[0] - n) * CB;
      __builtin_amdgcn_raw_buffer_store_b128(seal(__builtin_bit_cast(f32x4, u32x4{tk << 8, 0u, 0u, 0u}), salt), rw, (sample && lane == 0) ? WS_TS + k * 512 + ((j >> 3) * 8 + xcd) * 16 : OOB, 0, AUX_SC1);
#pragma unroll 1
      for (unsigned spin = 0; spin < SPIN_CAP; ++spin) { if (*(volatile LAS int*)(L + L_CK) >= n + CL) break; __builtin_amdgcn_s_sleep(4); }
      const u32x4 v = __builtin_amdgcn_raw_buffer_load_b128(rw, lane < 32 ? WS_TS + k * 512 + lane * 16 : OOB, 0, AUX_SC1);
      const bool ok = lane < 32 && sum4(v) == salt;
      const int cnt = __builtin_popcountll(__builtin_amdgcn_ballot_w64(ok));
      const float mean = wave_sum(ok ? (float)((int)((v[0] & 0xffffff00u) - (tk << 8)) >> 8) : 0.f) * __builtin_amdgcn_rcpf((float)cnt);
      if (cnt >= 16) *(volatile LAS u32x2*)(L + L_LINE) = u32x2{1u, tk + (int)mean + CD - n * CB};
    }
    if (TA) {
#pragma unroll 1
      for (unsigned spin = 0; spin < SPIN_CAP; ++spin) { if (*(volatile LAS int*)(L + L_CK) >= TA) break; __builtin_amdgcn_s_sleep(4); }
      const unsigned t0 = __builtin_amdgcn_raw_buffer_load_b32(rw, lane < 16 ? box + BX_PA + lane * 128 : (TS_ && lane < 20) ? box + BX_ZB + (lane & 3) * 128
                                                                 : (TS_ && lane < 24) ? WS_BOX + ((j >> 2) * 32 + (j & 3) * 8 + xcd) * BOX + BX_SB + (lane & 3) * 128 : OOB, 0, AUX_SC1);
      asm volatile("" :: "v"(t0));
    }
  }
  if (wave >= 4) {
    unsigned dst1 = (wave == 7 || (ZE && wave == 6)) ? OOB : wave == 6 ? ((r < 8 && mlp) ? box + BX_ZB + (r >> 1) * 128 + (r & 1) * 64 + g * 16 : OOB)
                                                : WS_BOX + (akv * 32 + slot * 2 + ab) * BOX + BX_PA + ((wave & 1) * 64 + lane) * 16;
    const int hh = (wave & 1) * 2 + (lane >> 5);
    LAS unsigned char *lm = L + L_M + hh * 16, *la = L + L_A + hh * A_STR + (lane & 31) * 16;
    PIN(dst1); PIN(lm); PIN(la);
    asm volatile("" :: "s"(ws));
    BAR();
    if (wave < 6) {
      const f32x4 mm = *(LAS f32x4*)lm, ll = *(LAS f32x4*)(lm + (L_L - L_M));
      f32x4 a[4];
#pragma unroll
      for (int w = 0; w < 4; ++w) a[w] = *(LAS f32x4*)(la + w * 4 * A_STR);
      SCHED();
      const float M = max3(max3(mm[0], mm[1], mm[2]), mm[3], mm[3]);
      float wt[4];
#pragma unroll
      for (int w = 0; w < 4; ++w) wt[w] = __builtin_amdgcn_exp2f(mm[w] - M);
      const float Ls = fmaf(ll[3], wt[3], fmaf(ll[2], wt[2], fmaf(ll[1], wt[1], ll[0] * wt[0])));
      f32x4 o = a[0] * wt[0];
#pragma unroll
      for (int w = 1; w < 4; ++w) o = __builtin_elementwise_fma(a[w], f32x4{wt[w], wt[w], wt[w], wt[w]}, o);
      o *= __builtin_amdgcn_rcpf(Ls);
      const unsigned lse = __float_as_uint(M + __builtin_amdgcn_logf(Ls));
      const u32x4 w = __builtin_bit_cast(u32x4, o);
      const unsigned sw = sum4_top(w), so = (unsigned)__builtin_amdgcn_mov_dpp((int)sw, XOR1, 0xf, 0xf, true);
      zp = low_bytes(w, (lane & 1) ? lse : salt - sw - rotr(so + lse, 5));
    }
    __builtin_amdgcn_raw_buffer_store_b128(zp, rw, dst1, 0, AUX_SC1);
  }
  unsigned psrc = WS_BOX + (xcd * 32 + (lane >> 3) * 2 + (wave >> 2)) * BOX + BX_PA + (j >> 3) * 512 + (wave & 3) * 128 + (lane & 7) * 16;
  unsigned dst2 = r < 2 ? box + BX_SB + (wave & 3) * 128 + r * 64 + g * 16 : OOB;
  LAS unsigned char* lo = L + L_O + (wave >> 2) * 256 + ((wave & 3) * 32 + (lane & 7) * 4 + g) * 2;
  LAS unsigned char* lb = L + L_O + (r & 1) * 256 + g * 16;
  PIN(dst2); PIN(lo); PIN(lb);
  LAS unsigned char* const aw = L + L_WO + (8 * g + (r >> 2)) * 128 + (r & 3) * 8 + (wave & 3) * 32;
  u32x4 wa[4];
#pragma unroll
  for (int k = 0; k < 4; ++k) wa[k] = tr2(aw, k * 4096, k * 4096 + 512);
  {
    u32x4 v0, v1;
    int spin = SPIN_CAP;
    unsigned even = (lane & 1) - 1u;
    PIN(even);
    if (PD) __builtin_amdgcn_s_sleep(PD);
#pragma unroll 1
    do {
      FENCE();
      v0 = __builtin_amdgcn_raw_buffer_load_b128(rw, psrc, 0, AUX_SC1);
      v1 = __builtin_amdgcn_raw_buffer_load_b128(rw, psrc + 16 * BOX, 0, AUX_SC1);
      const unsigned s0 = sum4(v0), s1 = sum4(v1);
      const unsigned c0 = s0 + rotr((unsigned)__builtin_amdgcn_mov_dpp((int)s0, XOR1, 0xf, 0xf, true), 5), c1 = s1 + rotr((unsigned)__builtin_amdgcn_mov_dpp((int)s1, XOR1, 0xf, 0xf, true), 5);
      if (__builtin_amdgcn_ballot_w64((((c0 ^ salt) | (c1 ^ salt)) & even) != 0) == 0) break;
      if (PSL) __builtin_amdgcn_s_sleep(PSL);
      GIVE_UP(even);
    } while (true);
#define LOWB(v) __uint_as_float(__builtin_amdgcn_perm(__builtin_amdgcn_perm(v[1], v[2], 0x04000000u), __builtin_amdgcn_perm(v[3], v[0], 0x00000400u), 0x07060100u))
    const float e0 = dpp<ODD>(LOWB(v0)), e1 = dpp<ODD>(LOWB(v1));
    const float M = xrow_max(max3(max3(e0, e1, dpp<ROR8>(e0)), dpp<ROR8>(e1), e0));
    const float w0 = __builtin_amdgcn_exp2f(e0 - M), w1_ = __builtin_amdgcn_exp2f(e1 - M);
    float Ws = w0 + w1_; Ws += dpp<ROR8>(Ws); Ws = xrow_sum(Ws);
    float o = rows_reduce4(__builtin_elementwise_fma(vals(v1), f32x4{w1_, w1_, w1_, w1_}, vals(v0) * w0));
    o += dpp<ROR8>(o);
    o *= __builtin_amdgcn_rcpf(Ws);
    *(LAS u16*)lo = (u16)cvtpk(o, o);
  }
  BAR();
  if (wave < 4) {
    u32x4 ob[4];
#pragma unroll
    for (int k = 0; k < 4; ++k) ob[k] = *(LAS u32x4*)(lb + k * 64);
    SCHED();
    const f32x4 z4 = {0.f, 0.f, 0.f, 0.f};
    const f32x4 s = mfma16(wa[1], ob[1], mfma16(wa[0], ob[0], z4)) + mfma16(wa[3], ob[3], mfma16(wa[2], ob[2], z4));
    __builtin_amdgcn_raw_buffer_store_b128(seal(s, salt), rw, dst2, 0, AUX_SC1);
  }
  if (wave == 6 && j < 16) {
    const unsigned in = (j >> 2) * 128 + (lane & 1) * 64 + (j & 3) * 16;
    const unsigned zsrc = lane < 56 ? WS_BOX + (xcd * 32 + (lane >> 1)) * BOX + BX_ZB + in : OOB;
    const unsigned ssrc = WS_BOX + ((lane >> 3) * 32 + ((lane >> 1) & 3) * 8 + xcd) * BOX + BX_SB + in;
    u32x4 v;
    int spin = SPIN_CAP;
    unsigned need = lane < 56 ? ~0u : 0u;
#pragma unroll 1
    do {
      FENCE();
      v = __builtin_amdgcn_raw_buffer_load_b128(rw, zsrc, 0, AUX_SC1);
      if (__builtin_amdgcn_ballot_w64(((sum4(v) ^ salt) & need) != 0) == 0) break;
      GIVE_UP(need);
    } while (true);
    float z = rows_reduce4(vals(v));
    z += dpp<XOR2>(z); z += dpp<ROR4>(z); z += dpp<ROR8>(z);
    z = rbf(rbf(z) + bias2);
    float gate = rbf(z * __builtin_amdgcn_rcpf(1.f + __builtin_amdgcn_exp2f(-LOG2E * z)));
    unsigned odst = r < 2 ? (r * 512 + xcd * 64 + j * 4 + g) * 2 : OOB;
    PIN(gate); PIN(odst);
    spin = SPIN_CAP; need = ~0u;
    if (SD) __builtin_amdgcn_s_sleep(SD);
#pragma unroll 1
    do {
      FENCE();
      v = __builtin_amdgcn_raw_buffer_load_b128(rw, ssrc, 0, AUX_SC1);
      if (__builtin_amdgcn_ballot_w64(((sum4(v) ^ salt) & need) != 0) == 0) break;
      if (SSL) __builtin_amdgcn_s_sleep(SSL);
      GIVE_UP(need);
    } while (true);
    float s = rows_reduce4(vals(v));
    s += dpp<XOR2>(s); s += dpp<ROR4>(s); s += dpp<ROR8>(s);
    const float res = xres + rbf(rbf(s) * gate);
    const auto rout = __builtin_amdgcn_make_buffer_rsrc(out, 0, 2048, RS3);
    __builtin_amdgcn_raw_buffer_store_b16((u16)cvtpk(res, res), rout, odst, 0, 0);
    __builtin_amdgcn_raw_buffer_store_b64(u32x2{(unsigned)gnext, (unsigned)(gnext >> 32)}, rw, (blockIdx.x == 0 && lane == 0) ? WS_G : OOB, 0, AUX_SC1);
  }
}

extern "C" void kernel_launch(void* const* d_in, const int* in_sizes, int n_in, void* d_out, int out_size, void* d_ws, size_t ws_size, hipStream_t stream) {
  fused_decode<<<256, 512, 0, stream>>>((const u16*)d_in[0], (const u16*)d_in[1], (const u16*)d_in[2], (const u16*)d_in[3], (const u16*)d_in[4],
                                        (const u16*)d_in[6], (const u16*)d_in[7], (const u16*)d_in[8], (const u16*)d_in[9], (u16*)d_out, (unsigned char*)d_ws);
}
```

```cpp
#include <hip/hip_runtime.h>
#include <stdint.h>

typedef unsigned short u16;
typedef unsigned long long u64;
typedef unsigned u32x2 __attribute__((ext_vector_type(2)));
typedef unsigned u32x4 __attribute__((ext_vector_type(4)));
typedef float f32x2 __attribute__((ext_vector_type(2)));
typedef float f32x4 __attribute__((ext_vector_type(4)));
typedef short s16x4 __attribute__((ext_vector_type(4)));
typedef short bf16x8 __attribute__((ext_vector_type(8)));
typedef __bf16 bf16x2_t __attribute__((ext_vector_type(2)));
#define GAS __attribute__((address_space(1)))
#define LAS __attribute__((address_space(3)))
#define CAS __attribute__((address_space(4)))

#define WK 5
#define WE 8
#define TE 14
#define PW 10
#define P0 1
#define LK 28
#define CB 112
#define CS 12
#define CL 2
#define TA 27
#define PD 16
#define PSL 2
constexpr int AH = WK + 1;
static_assert(AH <= 8 && WE >= 8 && (TE == 12 || TE == 14) && P0 < PW - 1 && PW <= 16 && LK >= 16 && LK <= 32 && TA < 16 + TE, "");

constexpr int SK = 32768;
constexpr int AUX_SC1 = 16, AUX_NT = 2, RS3 = 0x00020000;
constexpr unsigned WS_G = 0, WS_QG = 4096, WS_HG = WS_QG + 8 * 16384, WS_TS = WS_HG + 224 * 512, WS_BOX = WS_TS + 16 * 512;
constexpr unsigned BOX = 3072 + 128, BX_PA = 0, BX_SB = 2048, BX_ZB = 2560, WS_END = WS_BOX + 256 * BOX;
constexpr int L_WQ = 0, L_WO = 16384, L_W1 = 32768, L_W2 = 40960, L_XN = 49152, L_Y = L_XN + 2048, L_Q = 53248, L_H = 54272, L_O = 54528,
              L_M = 55040, L_L = 55104, L_PRG = 55168, L_F1 = 55184, L_CK = 55216, L_LINE = 55224, L_A = 55296, A_STR = 528, L_S = L_A + 16 * A_STR, L_END = L_S + 32768;
constexpr unsigned OOB = 0x80000000u;
constexpr unsigned SPIN_CAP = 1u << 19;
constexpr float LOG2E = 1.4426950408889634f, C2 = 0.08838834764831845f * LOG2E;

#define SCHED() __builtin_amdgcn_sched_barrier(0)

__device__ __forceinline__ f32x4 mfma16(u32x4 a, u32x4 b, f32x4 c) {
  return __builtin_amdgcn_mfma_f32_16x16x32_bf16(__builtin_bit_cast(bf16x8, a), __builtin_bit_cast(bf16x8, b), c, 0, 0, 0);
}
__device__ __forceinline__ unsigned cvtpk(float lo, float hi) { return __builtin_bit_cast(unsigned, __builtin_convertvector(f32x2{lo, hi}, bf16x2_t)); }
__device__ __forceinline__ float blo(unsigned u) { return __uint_as_float(u << 16); }
__device__ __forceinline__ float bhi(unsigned u) { return __uint_as_float(u & 0xffff0000u); }
__device__ __forceinline__ float rbf(float x) { return blo(cvtpk(x, x)); }
template <int CTRL> __device__ __forceinline__ float dpp(float x) {
  return __builtin_bit_cast(float, __builtin_amdgcn_mov_dpp(__builtin_bit_cast(int, x), CTRL, 0xf, 0xf, true));
}
constexpr int XOR1 = 0xB1, XOR2 = 0x4E, ROR4 = 0x124, ROR8 = 0x128, HMIR = 0x141, ODD = 0xF5;
__device__ __forceinline__ float max3(float a, float b, float c) { float d; asm("v_max3_f32 %0, %1, %2, %3" : "=v"(d) : "v"(a), "v"(b), "v"(c)); return d; }
__device__ __forceinline__ float xrow_max(float x) {
  auto s = __builtin_amdgcn_permlane16_swap(__float_as_uint(x), __float_as_uint(x), false, false);
  x = max3(__uint_as_float(s[0]), __uint_as_float(s[1]), __uint_as_float(s[1]));
  auto t = __builtin_amdgcn_permlane32_swap(__float_as_uint(x), __float_as_uint(x), false, false);
  return max3(__uint_as_float(t[0]), __uint_as_float(t[1]), __uint_as_float(t[1]));
}
__device__ __forceinline__ float xrow_sum(float x) {
  auto s = __builtin_amdgcn_permlane16_swap(__float_as_uint(x), __float_as_uint(x), false, false);
  x = __uint_as_float(s[0]) + __uint_as_float(s[1]);
  auto t = __builtin_amdgcn_permlane32_swap(__float_as_uint(x), __float_as_uint(x), false, false);
  return __uint_as_float(t[0]) + __uint_as_float(t[1]);
}
__device__ __forceinline__ float wave_sum(float x) {
  x += dpp<XOR1>(x); x += dpp<XOR2>(x); x += dpp<ROR4>(x); x += dpp<ROR8>(x); return xrow_sum(x);
}
__device__ __forceinline__ float rows_reduce4(f32x4 v) {
  auto a = __builtin_amdgcn_permlane32_swap(__float_as_uint(v[0]), __float_as_uint(v[2]), false, false);
  auto b = __builtin_amdgcn_permlane32_swap(__float_as_uint(v[1]), __float_as_uint(v[3]), false, false);
  const float u0 = __uint_as_float(a[0]) + __uint_as_float(a[1]), u1 = __uint_as_float(b[0]) + __uint_as_float(b[1]);
  auto c = __builtin_amdgcn_permlane16_swap(__float_as_uint(u0), __float_as_uint(u1), false, false);
  return __uint_as_float(c[0]) + __uint_as_float(c[1]);
}
__device__ __forceinline__ u32x4 ln8(u32x4 v) {
  float f[8];
#pragma unroll
  for (int i = 0; i < 4; ++i) { f[2 * i] = blo(v[i]); f[2 * i + 1] = bhi(v[i]); }
  const float mean = wave_sum(((f[0] + f[1]) + (f[2] + f[3])) + ((f[4] + f[5]) + (f[6] + f[7]))) * (1.f / 512.f);
  float ss = 0.f;
#pragma unroll
  for (int i = 0; i < 8; ++i) { const float d = f[i] - mean; ss = fmaf(d, d, ss); }
  const float var = wave_sum(ss) * (1.f / 512.f);
  const float mb = rbf(mean), inv = __builtin_amdgcn_rcpf(rbf(__builtin_amdgcn_sqrtf(rbf(rbf(var) + 1e-5f))));
  u32x4 o;
#pragma unroll
  for (int i = 0; i < 4; ++i) { const unsigned c = cvtpk(f[2 * i] - mb, f[2 * i + 1] - mb); o[i] = cvtpk(blo(c) * inv, bhi(c) * inv); }
  return o;
}
__device__ __forceinline__ u32x4 tr2(LAS unsigned char* p, int o0, int o1) {
  const u32x2 a = __builtin_bit_cast(u32x2, __builtin_amdgcn_ds_read_tr16_b64_v4i16((LAS s16x4*)(p + o0)));
  const u32x2 b = __builtin_bit_cast(u32x2, __builtin_amdgcn_ds_read_tr16_b64_v4i16((LAS s16x4*)(p + o1)));
  return u32x4{a[0], a[1], b[0], b[1]};
}
__device__ __forceinline__ f32x4 gemv64(LAS unsigned char* img, LAS unsigned char* bp, int g, int r) {
  LAS unsigned char* const a = img + (8 * g + (r >> 2)) * 128 + (r & 3) * 8;
  const u32x4 b0 = *(LAS u32x4*)(bp + g * 16), b1 = *(LAS u32x4*)(bp + 64 + g * 16);
  f32x4 z[4];
#pragma unroll
  for (int n = 0; n < 4; ++n) {
    z[n] = mfma16(tr2(a, n * 32, n * 32 + 512), b0, f32x4{0.f, 0.f, 0.f, 0.f});
    z[n] = mfma16(tr2(a, 4096 + n * 32, 4096 + n * 32 + 512), b1, z[n]);
  }
  return (r & 4) ? ((r & 2) ? z[3] : z[2]) : ((r & 2) ? z[1] : z[0]);
}
#define BAR() asm volatile("s_waitcnt lgkmcnt(0)\n\ts_barrier" ::: "memory")
#define FENCE() asm volatile("" ::: "memory")
#define VMW(N) do { __builtin_amdgcn_s_waitcnt(0x0F70 | ((N) & 15) | (((N) >> 4) << 14)); SCHED(); } while (0)

__device__ __forceinline__ unsigned rotr(unsigned x, unsigned n) { return __builtin_amdgcn_alignbit(x, x, n); }
__device__ __forceinline__ unsigned sum4(u32x4 w) { return w[0] + rotr(w[1], 8) + rotr(w[2], 16) + rotr(w[3], 24); }
__device__ __forceinline__ unsigned sum4_top(u32x4 w) {
  return (w[0] & 0xffffff00u) + __builtin_amdgcn_perm(0u, w[1], 0x0c030201u) + __builtin_amdgcn_perm(0u, w[2], 0x010c0302u) + __builtin_amdgcn_perm(0u, w[3], 0x02010c03u);
}
__device__ __forceinline__ u32x4 low_bytes(u32x4 w, unsigned d) {
  return u32x4{__builtin_amdgcn_perm(w[0], d, 0x07060500u), __builtin_amdgcn_perm(w[1], d, 0x07060503u), __builtin_amdgcn_perm(w[2], d, 0x07060502u), __builtin_amdgcn_perm(w[3], d, 0x07060501u)};
}
__device__ __forceinline__ u32x4 seal(f32x4 v, unsigned salt) { const u32x4 w = __builtin_bit_cast(u32x4, v); return low_bytes(w, salt - sum4_top(w)); }
__device__ __forceinline__ f32x4 vals(u32x4 w) { return __builtin_bit_cast(f32x4, w & 0xffffff00u); }
__device__ __forceinline__ f32x4 halves_sum(f32x4 v) {
  auto a = __builtin_amdgcn_permlane32_swap(__float_as_uint(v[0]), __float_as_uint(v[0]), false, false), b = __builtin_amdgcn_permlane32_swap(__float_as_uint(v[1]), __float_as_uint(v[1]), false, false);
  auto c = __builtin_amdgcn_permlane32_swap(__float_as_uint(v[2]), __float_as_uint(v[2]), false, false), d = __builtin_amdgcn_permlane32_swap(__float_as_uint(v[3]), __float_as_uint(v[3]), false, false);
  return f32x4{__uint_as_float(a[0]) + __uint_as_float(a[1]), __uint_as_float(b[0]) + __uint_as_float(b[1]), __uint_as_float(c[0]) + __uint_as_float(c[1]), __uint_as_float(d[0]) + __uint_as_float(d[1])};
}
#define SALT(g) ((unsigned)(g) * 0x9E3779B1u + 0x7F4A7C15u)
#define GIVE_UP(mask) do { --spin; mask = spin > 0 ? mask : 0u; asm volatile("" : "+v"(mask), "+v"(spin)); } while (0)
#define PIN(x) asm volatile("" : "+v"(x))

extern "C" __global__ void __launch_bounds__(512, 2)
fused_decode(const u16* a_x, const u16* a_k, const u16* a_v, const u16* a_wq, const u16* a_wo, const u16* a_w1, const u16* a_b1, const u16* a_w2, const u16* a_b2,
             u16* a_out, unsigned char* a_ws) {
  __shared__ __attribute__((aligned(16))) unsigned char smem[L_END];
  LAS unsigned char* const L = (LAS unsigned char*)smem;
#define ARGS() const CAS u64* kp_ = (const CAS u64*)__builtin_amdgcn_kernarg_segment_ptr(); asm volatile("" : "+s"(kp_)); \
  const u16* const x = (const u16*)kp_[0]; const u16* const Kc = (const u16*)kp_[1]; const u16* const Vc = (const u16*)kp_[2]; const u16* const wq = (const u16*)kp_[3]; \
  const u16* const wo = (const u16*)kp_[4]; const u16* const w1 = (const u16*)kp_[5]; const u16* const b1 = (const u16*)kp_[6]; const u16* const w2 = (const u16*)kp_[7]; \
  const u16* const b2 = (const u16*)kp_[8]; u16* const out = (u16*)kp_[9]; unsigned char* const ws = (unsigned char*)kp_[10]; \
  const auto rw = __builtin_amdgcn_make_buffer_rsrc(ws, 0, WS_END, RS3); \
  (void)x; (void)Kc; (void)Vc; (void)wq; (void)wo; (void)w1; (void)b1; (void)w2; (void)b2; (void)out; (void)rw
  const int lane = threadIdx.x & 63;
  int wave = __builtin_amdgcn_readfirstlane(threadIdx.x >> 6);
  const int xcd = blockIdx.x & 7, j = blockIdx.x >> 3, lw = xcd * 32 + j;
  const int g = lane >> 4, r = lane & 15;
  const unsigned box = WS_BOX + lw * BOX;
  const int pr = (xcd & 3) * 32 + j, ab = (pr >> 3) & 1, akv = pr >> 4, slot = (pr & 7) * 2 + (xcd >> 2);
  const bool mlp = j < 28;
  const int jm = mlp ? j : 0;
  float xres = 0.f, bias2 = 0.f;
  u32x4 zp = {0u, 0u, 0u, 0u};
  unsigned salt = 0;
  u64 gnext = 0;

  if (wave < 4) {
    ARGS();
    asm volatile("s_setprio 3" ::: "memory");
    SCHED();
    unsigned vq = (wave * 32 + (lane >> 3)) * 8192 + (lane & 7) * 16, v1 = (wave * 16 + (lane >> 3)) * 3584 + (lane & 7) * 16;
    unsigned vx = (wave & 1) * 1024 + lane * 16;
    unsigned qo = ((j & 3) * 128 * 4096 + xcd * 512 + (j >> 2) * 64) * 2, o1 = (xcd * 64 * 1792 + jm * 64) * 2, len1 = mlp ? 63u * 3584u + 128u : 0u;
    constexpr unsigned PSTR = 8192, WSTR = 2048;
    unsigned koff = wave * WSTR + (r >> 2) * 256 + g * 64 + (r & 3) * 16, voff = wave * WSTR + lane * 16, kvo = ((ab * 8 + akv) * SK + slot * 2048) * 256u;
    asm volatile("" : "+v"(vx), "+v"(vq), "+v"(v1), "+v"(koff), "+v"(voff), "+s"(qo), "+s"(o1), "+s"(len1), "+s"(kvo));
    {
      const u64 qb = (u64)wq + qo, b1_ = (u64)w1 + o1;
      const u32x4 rq = {(unsigned)qb, (unsigned)(qb >> 32) & 0xffffu, 127u * 8192u + 128u, (unsigned)RS3};
      const u32x4 r1 = {(unsigned)b1_, (unsigned)(b1_ >> 32) & 0xffffu, len1, (unsigned)RS3};
      const u32x4 rx = {(unsigned)(u64)x, (unsigned)((u64)x >> 32) & 0xffffu, 2048u, (unsigned)RS3};
      asm volatile("s_mov_b32 m0, %12\n\ts_nop 3\n\tbuffer_load_dwordx4 %10, %11, 0 offen lds\n\t"
                   "s_mov_b32 m0, %2\n\ts_nop 0\n\t"
                   "buffer_load_dwordx4 %0, %1, 0 offen nt lds\n\t"
                   "s_add_u32 m0, m0, 0x400\n\ts_nop 0\n\tbuffer_load_dwordx4 %0, %1, %3 offen nt lds\n\t"
                   "s_add_u32 m0, m0, 0x400\n\ts_nop 0\n\tbuffer_load_dwordx4 %0, %1, %4 offen nt lds\n\t"
                   "s_add_u32 m0, m0, 0x400\n\ts_nop 0\n\tbuffer_load_dwordx4 %0, %1, %5 offen nt lds\n\t"
                   "s_mov_b32 m0, %8\n\ts_nop 0\n\tbuffer_load_dwordx4 %6, %7, 0 offen nt lds\n\t"
                   "s_add_u32 m0, m0, 0x400\n\ts_nop 0\n\tbuffer_load_dwordx4 %6, %7, %9 offen nt lds"
                   :: "v"(vq), "s"(rq), "s"((unsigned)(size_t)(L + L_WQ) + wave * 4096), "s"(0x10000u), "s"(0x20000u), "s"(0x30000u),
                      "v"(v1), "s"(r1), "s"((unsigned)(size_t)(L + L_W1) + wave * 2048), "s"(8u * 3584u),
                      "v"(vx), "s"(rx), "s"((unsigned)(size_t)(L + L_XN) + wave * 1024) : "memory", "scc");
    }
    __builtin_amdgcn_s_setprio(0);
    const CAS u64* gp = (const CAS u64*)(ws + WS_G);
    asm volatile("" : "+s"(gp));
    const u64 gprev = *gp;
    const auto rk = __builtin_amdgcn_make_buffer_rsrc((char*)Kc + kvo, 0, 2048 * 256, RS3);
    const auto rv = __builtin_amdgcn_make_buffer_rsrc((char*)Vc + kvo, 0, 2048 * 256, RS3);
    u32x4 tb[4][8];
#define LOAD(B, I, RS, VO, T) do { tb[B][I] = __builtin_amdgcn_raw_buffer_load_b128(RS, (VO) + ((I) & 1) * 1024, (unsigned)(T) * 32768u + ((I) >> 1) * PSTR, AUX_NT); SCHED(); } while (0)
#define PRE(H) do { if ((H) == P0) asm volatile("s_waitcnt vmcnt(%0)\n\ts_barrier" :: "n"(6 + P0) : "memory");   \
    if ((H) > P0) VMW(PW - 1); \
    if ((H) == PW - 1) { FENCE(); *(volatile LAS int*)(L + L_F1 + wave * 4) = 1; SCHED(); }     \
    LOAD((H) >> 3, (H) & 7, rk, koff, (H) >> 3); } while (0)
#define PRE2(H) do { if ((H) < LK) PRE(H); } while (0)
#define PRE3(H) do { if ((H) >= LK) PRE(H); } while (0)
#define X8(M, H) do { M(H); M((H) + 1); M((H) + 2); M((H) + 3); M((H) + 4); M((H) + 5); M((H) + 6); M((H) + 7); } while (0)
    SCHED();
    X8(PRE, 0); X8(PRE, 8);
    { u64 g_ = gprev; asm volatile("" : "+s"(g_)); salt = SALT(g_ + 1); }
    X8(PRE2, 16); X8(PRE2, 24);
    const unsigned qsrc = WS_QG + akv * 16384 + (lane >> 5) * 4096 + (wave * 8 + ((lane >> 2) & 7)) * 128 + ab * 64 + (lane & 3) * 16;
    u32x4 qv[2];
    {
      const u32x4 rwq = {(unsigned)(u64)ws, (unsigned)((u64)ws >> 32) & 0xffffu, WS_END, (unsigned)RS3};
      asm volatile("s_waitcnt vmcnt(%5)\n\tbuffer_load_dwordx4 %0, %2, %3, 0 offen sc1\n\tbuffer_load_dwordx4 %1, %2, %3, %4 offen sc1"
                   : "=&v"(qv[0]), "=&v"(qv[1]) : "v"(qsrc), "s"(rwq), "s"(8192u), "n"(PW - 2) : "memory");
    }
    X8(PRE3, 16); X8(PRE3, 24);
    {
      const u64 b2_ = (u64)(w2 + jm * 64 * 512 + xcd * 64), bo = (u64)(wo + (xcd * 512 + (j >> 3) * 128) * 512 + (j & 7) * 64);
      const u32x4 r2 = {(unsigned)b2_, (unsigned)(b2_ >> 32) & 0xffffu, mlp ? 63u * 1024u + 128u : 0u, (unsigned)RS3};
      const u32x4 ro = {(unsigned)bo, (unsigned)(bo >> 32) & 0xffffu, 127u * 1024u + 128u, (unsigned)RS3};
#define DMA2(V, R, LB, S0, S1) asm volatile("s_waitcnt vmcnt(%5)\n\ts_mov_b32 m0, %2\n\ts_nop 0\n\tbuffer_load_dwordx4 %0, %1, %3 offen nt lds\n\t" \
                     "s_add_u32 m0, m0, 0x400\n\ts_waitcnt vmcnt(%5)\n\tbuffer_load_dwordx4 %0, %1, %4 offen nt lds" :: "v"(V), "s"(R), "s"(LB), "s"(S0), "s"(S1), "n"(PW - 1) : "memory", "scc")
      const unsigned v2 = (wave * 16 + (lane >> 3)) * 1024 + (lane & 7) * 16, vo = (lane >> 3) * 8192 + wave * 2048 + (lane & 7) * 16;
      DMA2(v2, r2, (unsigned)(size_t)(L + L_W2) + wave * 2048, 0u, 8192u);
      DMA2(vo, ro, (unsigned)(size_t)(L + L_WO) + wave * 4096, 0u, 65536u);
      DMA2(vo, ro, (unsigned)(size_t)(L + L_WO) + wave * 4096 + 2048, 1024u, 65536u + 1024u);
    }
    asm volatile("s_waitcnt vmcnt(%2)" : "+v"(qv[0]), "+v"(qv[1]) : "n"(32 - LK + 6));
    {
#define QBAD() (__builtin_amdgcn_ballot_w64(((sum4(qv[0]) ^ salt) | (sum4(qv[1]) ^ salt)) != 0) != 0)
      if (__builtin_expect(QBAD(), 0)) {
#pragma unroll 1
        for (unsigned spin = 0; spin < SPIN_CAP; ++spin) {
          FENCE();
          qv[0] = __builtin_amdgcn_raw_buffer_load_b128(rw, qsrc, 0, AUX_SC1); qv[1] = __builtin_amdgcn_raw_buffer_load_b128(rw, qsrc + 8192, 0, AUX_SC1);
          if (!QBAD()) break;
        }
      }
      const f32x4 q4 = halves_sum(vals(qv[0]) + vals(qv[1]));
      *(LAS u32x2*)(L + L_Q + wave * 256 + (lane & 31) * 8) = u32x2{cvtpk(q4[0], q4[1]), cvtpk(q4[2], q4[3])};
    }
    BAR();
    const int head = (r & 3) ^ ((r & 4) ? 3 : 0);
    const u32x4 qd = *(LAS u32x4*)(L + L_Q + head * 256 + g * 64 + (r >> 2) * 16);
    LAS unsigned char* sr = L + L_S + wave * 8192 + g * 128 + head * 32;
    LAS unsigned char* sw = sr + (r & 4) * 4 + (r >> 3) * 4;
    float m = -3.0e38f, y[4];
#define QP(B, P) do { const f32x4 z4 = {0.f, 0.f, 0.f, 0.f}, da = mfma16(tb[B][2 * (P)], qd, z4), db = mfma16(tb[B][2 * (P) + 1], qd, z4); \
    const float a0 = (r & 4) ? da[1] : da[0], a1 = (r & 4) ? da[3] : da[2], b0 = (r & 4) ? db[1] : db[0], b1 = (r & 4) ? db[3] : db[2]; \
    y[P] = ((r & 8) ? b1 : a0) + dpp<ROR8>((r & 8) ? a1 : b0); } while (0)
#define QE(T) do { const float z0 = ((r & 4) ? y[2] : y[0]) + dpp<HMIR>((r & 4) ? y[0] : y[2]), z1 = ((r & 4) ? y[3] : y[1]) + dpp<HMIR>((r & 4) ? y[1] : y[3]); \
    m = fmaxf(m, fmaxf(z0, z1)); *(LAS float*)(sw + (T) * 512) = z0; *(LAS float*)(sw + (T) * 512 + 8) = z1; } while (0)
#define QS(B, T) do { QP(B, 0); QP(B, 1); QP(B, 2); QP(B, 3); QE(T); SCHED(); } while (0)
#define CATCH(I) do { if ((I) < AH) LOAD(0, I, rk, koff, 4); } while (0)
    QS(0, 0); CATCH(0); CATCH(1); CATCH(2); CATCH(3); CATCH(4); CATCH(5); CATCH(6); CATCH(7);
    QS(1, 1); QS(2, 2); QS(3, 3);
    VMW(AH);
    *(volatile LAS int*)(L + L_PRG + wave * 4) = 1;
    LAS unsigned char* la = L + L_A + (wave * 4 + r) * A_STR + g * 16;
    PIN(la);
    int lag = 0;
#define WAITK() do { asm volatile("s_cmp_gt_i32 %0, 0\n\ts_cbranch_scc1 .Lw%=\n\ts_waitcnt vmcnt(%1)\n.Lw%=:\n\ts_waitcnt vmcnt(%2)" :: "s"(lag), "n"(WK - 1), "n"(WK) : "scc"); SCHED(); } while (0)
#define CTL0() const u64 now64_ = __builtin_amdgcn_s_memrealtime(); const u32x2 lv_ = *(volatile LAS u32x2*)(L + L_LINE); SCHED()
#define CTL1(N) do { asm volatile("" :: "s"(now64_)); const unsigned now_ = (unsigned)now64_; lag = __builtin_amdgcn_readfirstlane((now_ - lv_[1] - (N) * CB) & lv_[0]); \
    if (wave == 0) *(volatile LAS u32x2*)(L + L_CK) = u32x2{(unsigned)(N), now_}; SCHED(); } while (0)
#define STEP(H, RS, VO, RN, VN, TN) do { WAITK(); \
    if ((H) + AH < 16) LOAD(((H) + AH) >> 3, ((H) + AH) & 7, RS, VO, t + (((H) + AH) >> 3)); else LOAD(0, ((H) + AH) & 7, RN, VN, TN); } while (0)
#define STEP2(H, ...) do { STEP(H, __VA_ARGS__); STEP((H) + 1, __VA_ARGS__); } while (0)
#define KT(B, ...) do { CTL0(); STEP2(8 * (B), __VA_ARGS__); CTL1(t + (B)); QP(B, 0); SCHED(); STEP2(8 * (B) + 2, __VA_ARGS__); QP(B, 1); SCHED(); \
    STEP2(8 * (B) + 4, __VA_ARGS__); QP(B, 2); SCHED(); STEP2(8 * (B) + 6, __VA_ARGS__); QP(B, 3); QE(t + (B)); SCHED(); } while (0)
#pragma unroll 1
    for (int t = 4; t < 16; t += 2) {
      const bool last = t == 14;
      const auto rn = __builtin_amdgcn_make_buffer_rsrc((last ? (char*)Vc : (char*)Kc) + kvo, 0, 2048 * 256, RS3);
      const unsigned vn = last ? voff : koff, tn = (t + 2) & 15;
      KT(0, rk, koff, rn, vn, tn); KT(1, rk, koff, rn, vn, tn);
    }
    m = fmaxf(m, dpp<HMIR>(m)); m = fmaxf(m, dpp<ROR8>(m));
    const float mc = xrow_max(m) * C2;
    float l = 0.f;
    f32x4 acc[8];
#pragma unroll
    for (int e = 0; e < 8; ++e) acc[e] = f32x4{0.f, 0.f, 0.f, 0.f};
    u32x4 pf, vt[8];
#define SP(T) do { const f32x4 s0 = *(LAS f32x4*)(sr + (T) * 512), s1 = *(LAS f32x4*)(sr + (T) * 512 + 16); float p[8]; \
    _Pragma("unroll") for (int i = 0; i < 4; ++i) { p[i] = __builtin_amdgcn_exp2f(fmaf(s0[i], C2, -mc)); p[4 + i] = __builtin_amdgcn_exp2f(fmaf(s1[i], C2, -mc)); } \
    l += ((p[0] + p[1]) + (p[2] + p[3])) + ((p[4] + p[5]) + (p[6] + p[7])); \
    pf = u32x4{cvtpk(p[0], p[1]), cvtpk(p[2], p[3]), cvtpk(p[4], p[5]), cvtpk(p[6], p[7])}; SCHED(); } while (0)
#define VP(B, P) do { _Pragma("unroll") for (int w = 0; w < 4; ++w) { vt[2 * w][P] = __builtin_amdgcn_perm(tb[B][2 * (P) + 1][w], tb[B][2 * (P)][w], 0x05040100u); \
      vt[2 * w + 1][P] = __builtin_amdgcn_perm(tb[B][2 * (P) + 1][w], tb[B][2 * (P)][w], 0x07060302u); } } while (0)
#define VE() do { _Pragma("unroll") for (int e = 0; e < 8; ++e) acc[e] = mfma16(vt[e], pf, acc[e]); SCHED(); } while (0)
#define VT(B, ...) do { CTL0(); STEP2(8 * (B), __VA_ARGS__); CTL1(16 + t + (B)); VP(B, 0); SP(t + (B)); STEP2(8 * (B) + 2, __VA_ARGS__); VP(B, 1); SCHED(); \
    STEP2(8 * (B) + 4, __VA_ARGS__); VP(B, 2); SCHED(); STEP2(8 * (B) + 6, __VA_ARGS__); VP(B, 3); VE(); } while (0)
#pragma unroll 1
    for (int t = 0; t < TE; t += 2) { VT(0, rv, voff, rv, voff, t + 2); VT(1, rv, voff, rv, voff, t + 2); }
#define EL(N) do { if ((N) < 8 * (16 - TE)) { if ((N) >= WE) VMW(WE - 1); LOAD((N) >> 3, (N) & 7, rv, voff, TE + ((N) >> 3)); } } while (0)
#define EP(N) do { if ((N) >= AH && (N) < WE) EL(N); } while (0)
    X8(EP, 0); X8(EP, 8); X8(EP, 16); X8(EP, 24);
#define ET(B) do { if (TE + (B) < 16) { SP(TE + (B)); EL(WE + 8 * (B)); EL(WE + 8 * (B) + 1); VP(B, 0); SCHED(); EL(WE + 8 * (B) + 2); EL(WE + 8 * (B) + 3); VP(B, 1); SCHED(); \
      EL(WE + 8 * (B) + 4); EL(WE + 8 * (B) + 5); VP(B, 2); SCHED(); EL(WE + 8 * (B) + 6); EL(WE + 8 * (B) + 7); VP(B, 3); VE(); } } while (0)
    ET(0); ET(1); ET(2); ET(3);
    l = xrow_sum(l);
    if (r < 4) {
#pragma unroll
      for (int e = 0; e < 8; ++e) *(LAS f32x4*)(la + e * 64) = acc[e];
      *(LAS float*)(L + L_M + r * 16 + wave * 4) = mc; *(LAS float*)(L + L_L + r * 16 + wave * 4) = l;
    }
    BAR();
  }
  asm volatile("" : "+s"(wave));
  if (wave >= 4) {
    __builtin_amdgcn_s_setprio(2);
    ARGS();
    const int n = wave - 4;
    gnext = *(const CAS u64*)(ws + WS_G) + 1;
    *(volatile LAS int*)(L + L_PRG + n * 4) = 0; *(volatile LAS int*)(L + L_F1 + n * 4) = 0; *(volatile LAS int*)(L + L_F1 + 16 + n * 4) = 0; *(volatile LAS int*)(L + L_CK + n * 4) = 0;
    unsigned qdst = r < 2 ? WS_QG + xcd * 16384 + (j & 3) * 4096 + ((j >> 2) * 4 + n) * 128 + r * 64 + g * 16 : OOB;
    unsigned hdst = (r < 2 && mlp) ? WS_HG + (j * 8 + xcd) * 512 + n * 128 + r * 64 + g * 16 : OOB;
    PIN(qdst); PIN(hdst);
    BAR();
    u32x4 xr = *(LAS u32x4*)(L + L_XN + n * 1024 + lane * 16);
#pragma unroll 1
    for (int it = n >> 1; it >= 0; --it) xr = ln8(xr);
    *(LAS u32x4*)(L + L_XN + n * 1024 + lane * 16) = xr;
    salt = SALT(gnext);
    asm volatile("s_waitcnt lgkmcnt(0)" : "+s"(salt), "+s"(gnext) :: "memory");
    *(volatile LAS int*)(L + L_F1 + 16 + n * 4) = 1;
#pragma unroll 1
    for (unsigned spin = 0; spin < 16 * SPIN_CAP; ++spin) {
      const u32x4 p = *(volatile LAS u32x4*)(L + L_F1) & *(volatile LAS u32x4*)(L + L_F1 + 16);
      if ((p[0] & p[1] & p[2] & p[3]) != 0) break;
    }
    FENCE();
    const int ao = (8 * g + (r >> 2)) * 128 + (r & 3) * 8 + n * 32;
    LAS unsigned char* const bp = L + L_XN + (r & 1) * 1024 + g * 16;
    f32x4 qv = {0.f, 0.f, 0.f, 0.f}, hv = {0.f, 0.f, 0.f, 0.f};
#pragma unroll
    for (int k = 0; k < 4; ++k) qv = mfma16(tr2(L + L_WQ + ao, k * 4096, k * 4096 + 512), *(LAS u32x4*)(bp + (j & 3) * 256 + k * 64), qv);
#pragma unroll
    for (int k = 0; k < 2; ++k) hv = mfma16(tr2(L + L_W1 + ao, k * 4096, k * 4096 + 512), *(LAS u32x4*)(bp + 2048 + xcd * 128 + k * 64), hv);
    __builtin_amdgcn_raw_buffer_store_b128(seal(qv, salt), rw, qdst, 0, AUX_SC1);
    __builtin_amdgcn_raw_buffer_store_b128(seal(hv, salt), rw, hdst, 0, AUX_SC1);
    unsigned code_seen = 0;
    if (n == 3) {
      u64 pc; unsigned back, len;
      asm volatile("s_getpc_b64 %0\n.Lpc%=:\n\ts_mov_b32 %1, .Lpc%=-fused_decode\n\ts_mov_b32 %2, .Lfd_last-fused_decode" : "=s"(pc), "=s"(back), "=s"(len));
      code_seen = __builtin_amdgcn_raw_buffer_load_b128(__builtin_amdgcn_make_buffer_rsrc((void*)(pc - back), 0, len, RS3), lane < 40 ? j * 640 + lane * 16 : OOB, 0, 0)[0];
    }
    asm volatile("s_barrier" :: "v"(code_seen) : "memory");
  }
  asm volatile("" : "+s"(wave));
  if (wave == 6) {
    ARGS();
    __builtin_amdgcn_s_setprio(0);
#pragma unroll 1
    for (unsigned spin = 0; spin < SPIN_CAP; ++spin) {
      const u32x4 p = *(volatile LAS u32x4*)(L + L_PRG);
      if (min(min(p[0], p[1]), min(p[2], p[3])) != 0) break;
      __builtin_amdgcn_s_sleep(16);
    }
    SCHED();
    __builtin_amdgcn_s_setprio(3);
    const int hcol = ((lane >> 3) & 3) * 16 + (lane & 3) * 4;
    const u32x2 bb = *(const GAS u32x2*)(b1 + jm * 64 + hcol);
    const int ocol = xcd * 64 + (j & 15) * 4 + g;
    xres = blo(((const GAS u16*)x)[(lane & 1) * 512 + ocol]);
    bias2 = blo(((const GAS u16*)b2)[ocol]);
    u32x4 v[4];
#pragma unroll 1
    for (unsigned spin = 0; spin < SPIN_CAP; ++spin) {
      FENCE();
#pragma unroll
      for (int k = 0; k < 4; ++k) v[k] = __builtin_amdgcn_raw_buffer_load_b128(rw, WS_HG + jm * 4096 + k * 1024 + lane * 16, 0, AUX_SC1);
      if (__builtin_amdgcn_ballot_w64(((sum4(v[0]) ^ salt) | (sum4(v[1]) ^ salt) | (sum4(v[2]) ^ salt) | (sum4(v[3]) ^ salt)) != 0) == 0) break;
      __builtin_amdgcn_s_sleep(16);
    }
    SCHED();
    __builtin_amdgcn_s_setprio(0);
    const f32x4 hs = halves_sum((vals(v[0]) + vals(v[1])) + (vals(v[2]) + vals(v[3])));
    float hh[4];
#pragma unroll
    for (int i = 0; i < 4; ++i) {
      const float z = rbf(rbf(hs[i]) + ((i & 1) ? bhi(bb[i >> 1]) : blo(bb[i >> 1])));
      const float u = 0.7978845608028654f * (z + 0.044715f * z * z * z);
      hh[i] = z * __builtin_amdgcn_rcpf(1.f + __builtin_amdgcn_exp2f(-2.f * LOG2E * u));
    }
    *(LAS u32x2*)(L + L_H + ((lane >> 2) & 1) * 128 + hcol * 2) = u32x2{cvtpk(hh[0], hh[1]), cvtpk(hh[2], hh[3])};
    zp = seal(gemv64(L + L_W2, L + L_H + (r & 1) * 128, g, r), salt);
    PIN(zp); PIN(xres); PIN(bias2);
  }
  asm volatile("" : "+s"(wave));
  ARGS();
  if (wave == 7) {
    const bool sample = (j & 7) == ((xcd + 3 * (j >> 3)) & 7);
#pragma unroll 1
    for (int n = 4, k = 0; n + CL < 16 + TE; n += CS, ++k) {
      u32x2 ck;
#pragma unroll 1
      for (unsigned spin = 0; spin < SPIN_CAP; ++spin) { ck = *(volatile LAS u32x2*)(L + L_CK); if ((int)ck[0] >= n) break; __builtin_amdgcn_s_sleep(4); }
      const unsigned tk = ck[1] - (ck[0] - n) * CB;
      __builtin_amdgcn_raw_buffer_store_b128(seal(__builtin_bit_cast(f32x4, u32x4{tk << 8, 0u, 0u, 0u}), salt), rw, (sample && lane == 0) ? WS_TS + k * 512 + ((j >> 3) * 8 + xcd) * 16 : OOB, 0, AUX_SC1);
#pragma unroll 1
      for (unsigned spin = 0; spin < SPIN_CAP; ++spin) { if (*(volatile LAS int*)(L + L_CK) >= n + CL) break; __builtin_amdgcn_s_sleep(4); }
      const u32x4 v = __builtin_amdgcn_raw_buffer_load_b128(rw, lane < 32 ? WS_TS + k * 512 + lane * 16 : OOB, 0, AUX_SC1);
      const bool ok = lane < 32 && sum4(v) == salt;
      const int cnt = __builtin_popcountll(__builtin_amdgcn_ballot_w64(ok));
      const float mean = wave_sum(ok ? (float)((int)((v[0] & 0xffffff00u) - (tk << 8)) >> 8) : 0.f) * __builtin_amdgcn_rcpf((float)cnt);
      if (cnt >= 16) *(volatile LAS u32x2*)(L + L_LINE) = u32x2{~0u, tk + (int)mean - n * CB};
    }
#pragma unroll 1
    for (unsigned spin = 0; spin < SPIN_CAP; ++spin) { if (*(volatile LAS int*)(L + L_CK) >= TA) break; __builtin_amdgcn_s_sleep(4); }
    const unsigned t0 = __builtin_amdgcn_raw_buffer_load_b32(rw, lane < 16 ? box + BX_PA + lane * 128 : OOB, 0, AUX_SC1);
    asm volatile("" :: "v"(t0));
  }
  if (wave >= 4) {
    unsigned dst1 = wave == 7 ? OOB : wave == 6 ? ((r < 8 && mlp) ? box + BX_ZB + (r >> 1) * 128 + (r & 1) * 64 + g * 16 : OOB)
                                                : WS_BOX + (akv * 32 + slot * 2 + ab) * BOX + BX_PA + ((wave & 1) * 64 + lane) * 16;
    const int hh = (wave & 1) * 2 + (lane >> 5);
    LAS unsigned char *lm = L + L_M + hh * 16, *la = L + L_A + hh * A_STR + (lane & 31) * 16;
    PIN(dst1); PIN(lm); PIN(la);
    asm volatile("" :: "s"(ws));
    BAR();
    if (wave < 6) {
      const f32x4 mm = *(LAS f32x4*)lm, ll = *(LAS f32x4*)(lm + (L_L - L_M));
      f32x4 a[4];
#pragma unroll
      for (int w = 0; w < 4; ++w) a[w] = *(LAS f32x4*)(la + w * 4 * A_STR);
      SCHED();
      const float M = max3(max3(mm[0], mm[1], mm[2]), mm[3], mm[3]);
      float wt[4];
#pragma unroll
      for (int w = 0; w < 4; ++w) wt[w] = __builtin_amdgcn_exp2f(mm[w] - M);
      const float Ls = fmaf(ll[3], wt[3], fmaf(ll[2], wt[2], fmaf(ll[1], wt[1], ll[0] * wt[0])));
      f32x4 o = a[0] * wt[0];
#pragma unroll
      for (int w = 1; w < 4; ++w) o = __builtin_elementwise_fma(a[w], f32x4{wt[w], wt[w], wt[w], wt[w]}, o);
      o *= __builtin_amdgcn_rcpf(Ls);
      const unsigned lse = __float_as_uint(M + __builtin_amdgcn_logf(Ls));
      const u32x4 w = __builtin_bit_cast(u32x4, o);
      const unsigned sw = sum4_top(w), so = (unsigned)__builtin_amdgcn_mov_dpp((int)sw, XOR1, 0xf, 0xf, true);
      zp = low_bytes(w, (lane & 1) ? lse : salt - sw - rotr(so + lse, 5));
    }
    __builtin_amdgcn_raw_buffer_store_b128(zp, rw, dst1, 0, AUX_SC1);
  }
  unsigned psrc = WS_BOX + (xcd * 32 + (lane >> 3) * 2 + (wave >> 2)) * BOX + BX_PA + (j >> 3) * 512 + (wave & 3) * 128 + (lane & 7) * 16;
  unsigned dst2 = r < 2 ? box + BX_SB + (wave & 3) * 128 + r * 64 + g * 16 : OOB;
  LAS unsigned char* lo = L + L_O + (wave >> 2) * 256 + ((wave & 3) * 32 + (lane & 7) * 4 + g) * 2;
  LAS unsigned char* lb = L + L_O + (r & 1) * 256 + g * 16;
  PIN(dst2); PIN(lo); PIN(lb);
  LAS unsigned char* const aw = L + L_WO + (8 * g + (r >> 2)) * 128 + (r & 3) * 8 + (wave & 3) * 32;
  u32x4 wa[4];
#pragma unroll
  for (int k = 0; k < 4; ++k) wa[k] = tr2(aw, k * 4096, k * 4096 + 512);
  {
    u32x4 v0, v1;
    int spin = SPIN_CAP;
    unsigned even = (lane & 1) - 1u;
    PIN(even);
    __builtin_amdgcn_s_sleep(PD);
#pragma unroll 1
    do {
      FENCE();
      v0 = __builtin_amdgcn_raw_buffer_load_b128(rw, psrc, 0, AUX_SC1);
      v1 = __builtin_amdgcn_raw_buffer_load_b128(rw, psrc + 16 * BOX, 0, AUX_SC1);
      const unsigned s0 = sum4(v0), s1 = sum4(v1);
      const unsigned c0 = s0 + rotr((unsigned)__builtin_amdgcn_mov_dpp((int)s0, XOR1, 0xf, 0xf, true), 5), c1 = s1 + rotr((unsigned)__builtin_amdgcn_mov_dpp((int)s1, XOR1, 0xf, 0xf, true), 5);
      if (__builtin_amdgcn_ballot_w64((((c0 ^ salt) | (c1 ^ salt)) & even) != 0) == 0) break;
      __builtin_amdgcn_s_sleep(PSL);
      GIVE_UP(even);
    } while (true);
#define LOWB(v) __uint_as_float(__builtin_amdgcn_perm(__builtin_amdgcn_perm(v[1], v[2], 0x04000000u), __builtin_amdgcn_perm(v[3], v[0], 0x00000400u), 0x07060100u))
    const float e0 = dpp<ODD>(LOWB(v0)), e1 = dpp<ODD>(LOWB(v1));
    const float M = xrow_max(max3(max3(e0, e1, dpp<ROR8>(e0)), dpp<ROR8>(e1), e0));
    const float w0 = __builtin_amdgcn_exp2f(e0 - M), w1_ = __builtin_amdgcn_exp2f(e1 - M);
    float Ws = w0 + w1_; Ws += dpp<ROR8>(Ws); Ws = xrow_sum(Ws);
    float o = rows_reduce4(__builtin_elementwise_fma(vals(v1), f32x4{w1_, w1_, w1_, w1_}, vals(v0) * w0));
    o += dpp<ROR8>(o);
    o *= __builtin_amdgcn_rcpf(Ws);
    *(LAS u16*)lo = (u16)cvtpk(o, o);
  }
  BAR();
  if (wave < 4) {
    u32x4 ob[4];
#pragma unroll
    for (int k = 0; k < 4; ++k) ob[k] = *(LAS u32x4*)(lb + k * 64);
    SCHED();
    const f32x4 z4 = {0.f, 0.f, 0.f, 0.f};
    const f32x4 s = mfma16(wa[1], ob[1], mfma16(wa[0], ob[0], z4)) + mfma16(wa[3], ob[3], mfma16(wa[2], ob[2], z4));
    __builtin_amdgcn_raw_buffer_store_b128(seal(s, salt), rw, dst2, 0, AUX_SC1);
  }
  if (wave == 6 && j < 16) {
    const unsigned in = (j >> 2) * 128 + (lane & 1) * 64 + (j & 3) * 16;
    const unsigned zsrc = lane < 56 ? WS_BOX + (xcd * 32 + (lane >> 1)) * BOX + BX_ZB + in : OOB;
    const unsigned ssrc = WS_BOX + ((lane >> 3) * 32 + ((lane >> 1) & 3) * 8 + xcd) * BOX + BX_SB + in;
    u32x4 v;
    int spin = SPIN_CAP;
    unsigned need = lane < 56 ? ~0u : 0u;
#pragma unroll 1
    do {
      FENCE();
      v = __builtin_amdgcn_raw_buffer_load_b128(rw, zsrc, 0, AUX_SC1);
      if (__builtin_amdgcn_ballot_w64(((sum4(v) ^ salt) & need) != 0) == 0) break;
      GIVE_UP(need);
    } while (true);
    float z = rows_reduce4(vals(v));
    z += dpp<XOR2>(z); z += dpp<ROR4>(z); z += dpp<ROR8>(z);
    z = rbf(rbf(z) + bias2);
    float gate = rbf(z * __builtin_amdgcn_rcpf(1.f + __builtin_amdgcn_exp2f(-LOG2E * z)));
    unsigned odst = r < 2 ? (r * 512 + xcd * 64 + j * 4 + g) * 2 : OOB;
    PIN(gate); PIN(odst);
    spin = SPIN_CAP; need = ~0u;
#pragma unroll 1
    do {
      FENCE();
      v = __builtin_amdgcn_raw_buffer_load_b128(rw, ssrc, 0, AUX_SC1);
      if (__builtin_amdgcn_ballot_w64(((sum4(v) ^ salt) & need) != 0) == 0) break;
      GIVE_UP(need);
    } while (true);
    float s = rows_reduce4(vals(v));
    s += dpp<XOR2>(s); s += dpp<ROR4>(s); s += dpp<ROR8>(s);
    const float res = xres + rbf(rbf(s) * gate);
    const auto rout = __builtin_amdgcn_make_buffer_rsrc(out, 0, 2048, RS3);
    __builtin_amdgcn_raw_buffer_store_b16((u16)cvtpk(res, res), rout, odst, 0, 0);
    __builtin_amdgcn_raw_buffer_store_b64(u32x2{(unsigned)gnext, (unsigned)(gnext >> 32)}, rw, (blockIdx.x == 0 && lane == 0) ? WS_G : OOB, 0, AUX_SC1);
    asm volatile(".Lfd_last:");
  }
}

extern "C" void kernel_launch(void* const* d_in, const int* in_sizes, int n_in, void* d_out, int out_size, void* d_ws, size_t ws_size, hipStream_t stream) {
  fused_decode<<<256, 512, 0, stream>>>((const u16*)d_in[0], (const u16*)d_in[1], (const u16*)d_in[2], (const u16*)d_in[3], (const u16*)d_in[4],
                                        (const u16*)d_in[6], (const u16*)d_in[7], (const u16*)d_in[8], (const u16*)d_in[9], (u16*)d_out, (unsigned char*)d_ws);
}
```

```cpp
#include <hip/hip_runtime.h>
#include <stdint.h>

typedef unsigned short u16;
typedef unsigned long long u64;
typedef unsigned u32x2 __attribute__((ext_vector_type(2)));
typedef unsigned u32x4 __attribute__((ext_vector_type(4)));
typedef float f32x2 __attribute__((ext_vector_type(2)));
typedef float f32x4 __attribute__((ext_vector_type(4)));
typedef short s16x4 __attribute__((ext_vector_type(4)));
typedef short bf16x8 __attribute__((ext_vector_type(8)));
typedef __bf16 bf16x2_t __attribute__((ext_vector_type(2)));
#define GAS __attribute__((address_space(1)))
#define LAS __attribute__((address_space(3)))
#define CAS __attribute__((address_space(4)))

#define WK 5
#define WE 8
#define TE 14
#define PW 10
#define P0 1
#define LK 28
#define CB 112
#define CS 12
#define CL 2
#define PD 16
#define PSL 2
constexpr int AH = WK + 1;
static_assert((16 + TE - CL - 5) / CS < 4 && AH <= 8 && WE >= 8 && (TE == 12 || TE == 14) && P0 < PW - 1 && PW <= 16 && LK >= 16 && LK <= 32, "");

constexpr int SK = 32768;
constexpr int AUX_SC1 = 16, AUX_NT = 2, RS3 = 0x00020000;
constexpr unsigned WS_G = 0, WS_QG = 4096, WS_HG = WS_QG + 8 * 16384, WS_TS = WS_HG + 224 * 512, WS_BOX = WS_TS + 4 * 512;
constexpr unsigned BOX = 3072 + 128, BX_PA = 0, BX_SB = 2048, BX_ZB = 2560, WS_END = WS_BOX + 256 * BOX;
constexpr int L_WQ = 0, L_WO = 16384, L_W1 = 32768, L_W2 = 40960, L_XN = 49152, L_Y = L_XN + 2048, L_Q = 53248, L_H = 54272, L_O = 54528,
              L_M = 55040, L_L = 55104, L_PRG = 55168, L_F1 = 55184, L_CK = 55216, L_LINE = 55224, L_A = 55296, A_STR = 528, L_S = L_A + 16 * A_STR, L_END = L_S + 32768;
constexpr unsigned OOB = 0x80000000u;
constexpr unsigned SPIN_CAP = 1u << 19;
constexpr float LOG2E = 1.4426950408889634f, C2 = 0.08838834764831845f * LOG2E;

#define SCHED() __builtin_amdgcn_sched_barrier(0)

__device__ __forceinline__ f32x4 mfma16(u32x4 a, u32x4 b, f32x4 c) {
  return __builtin_amdgcn_mfma_f32_16x16x32_bf16(__builtin_bit_cast(bf16x8, a), __builtin_bit_cast(bf16x8, b), c, 0, 0, 0);
}
__device__ __forceinline__ unsigned cvtpk(float lo, float hi) { return __builtin_bit_cast(unsigned, __builtin_convertvector(f32x2{lo, hi}, bf16x2_t)); }
__device__ __forceinline__ float blo(unsigned u) { return __uint_as_float(u << 16); }
__device__ __forceinline__ float bhi(unsigned u) { return __uint_as_float(u & 0xffff0000u); }
__device__ __forceinline__ float rbf(float x) { return blo(cvtpk(x, x)); }
template <int CTRL> __device__ __forceinline__ float dpp(float x) {
  return __builtin_bit_cast(float, __builtin_amdgcn_mov_dpp(__builtin_bit_cast(int, x), CTRL, 0xf, 0xf, true));
}
constexpr int XOR1 = 0xB1, XOR2 = 0x4E, ROR4 = 0x124, ROR8 = 0x128, HMIR = 0x141, ODD = 0xF5;
__device__ __forceinline__ float max3(float a, float b, float c) { float d; asm("v_max3_f32 %0, %1, %2, %3" : "=v"(d) : "v"(a), "v"(b), "v"(c)); return d; }
__device__ __forceinline__ float xrow_max(float x) {
  auto s = __builtin_amdgcn_permlane16_swap(__float_as_uint(x), __float_as_uint(x), false, false);
  x = max3(__uint_as_float(s[0]), __uint_as_float(s[1]), __uint_as_float(s[1]));
  auto t = __builtin_amdgcn_permlane32_swap(__float_as_uint(x), __float_as_uint(x), false, false);
  return max3(__uint_as_float(t[0]), __uint_as_float(t[1]), __uint_as_float(t[1]));
}
__device__ __forceinline__ float xrow_sum(float x) {
  auto s = __builtin_amdgcn_permlane16_swap(__float_as_uint(x), __float_as_uint(x), false, false);
  x = __uint_as_float(s[0]) + __uint_as_float(s[1]);
  auto t = __builtin_amdgcn_permlane32_swap(__float_as_uint(x), __float_as_uint(x), false, false);
  return __uint_as_float(t[0]) + __uint_as_float(t[1]);
}
__device__ __forceinline__ float wave_sum(float x) {
  x += dpp<XOR1>(x); x += dpp<XOR2>(x); x += dpp<ROR4>(x); x += dpp<ROR8>(x); return xrow_sum(x);
}
__device__ __forceinline__ float rows_reduce4(f32x4 v) {
  auto a = __builtin_amdgcn_permlane32_swap(__float_as_uint(v[0]), __float_as_uint(v[2]), false, false);
  auto b = __builtin_amdgcn_permlane32_swap(__float_as_uint(v[1]), __float_as_uint(v[3]), false, false);
  const float u0 = __uint_as_float(a[0]) + __uint_as_float(a[1]), u1 = __uint_as_float(b[0]) + __uint_as_float(b[1]);
  auto c = __builtin_amdgcn_permlane16_swap(__float_as_uint(u0), __float_as_uint(u1), false, false);
  return __uint_as_float(c[0]) + __uint_as_float(c[1]);
}
__device__ __forceinline__ u32x4 ln8(u32x4 v) {
  float f[8];
#pragma unroll
  for (int i = 0; i < 4; ++i) { f[2 * i] = blo(v[i]); f[2 * i + 1] = bhi(v[i]); }
  const float mean = wave_sum(((f[0] + f[1]) + (f[2] + f[3])) + ((f[4] + f[5]) + (f[6] + f[7]))) * (1.f / 512.f);
  float ss = 0.f;
#pragma unroll
  for (int i = 0; i < 8; ++i) { const float d = f[i] - mean; ss = fmaf(d, d, ss); }
  const float var = wave_sum(ss) * (1.f / 512.f);
  const float mb = rbf(mean), inv = __builtin_amdgcn_rcpf(rbf(__builtin_amdgcn_sqrtf(rbf(rbf(var) + 1e-5f))));
  u32x4 o;
#pragma unroll
  for (int i = 0; i < 4; ++i) { const unsigned c = cvtpk(f[2 * i] - mb, f[2 * i + 1] - mb); o[i] = cvtpk(blo(c) * inv, bhi(c) * inv); }
  return o;
}
__device__ __forceinline__ u32x4 tr2(LAS unsigned char* p, int o0, int o1) {
  const u32x2 a = __builtin_bit_cast(u32x2, __builtin_amdgcn_ds_read_tr16_b64_v4i16((LAS s16x4*)(p + o0)));
  const u32x2 b = __builtin_bit_cast(u32x2, __builtin_amdgcn_ds_read_tr16_b64_v4i16((LAS s16x4*)(p + o1)));
  return u32x4{a[0], a[1], b[0], b[1]};
}
__device__ __forceinline__ f32x4 gemv64(LAS unsigned char* img, LAS unsigned char* bp, int g, int r) {
  LAS unsigned char* const a = img + (8 * g + (r >> 2)) * 128 + (r & 3) * 8;
  const u32x4 b0 = *(LAS u32x4*)(bp + g * 16), b1 = *(LAS u32x4*)(bp + 64 + g * 16);
  f32x4 z[4];
#pragma unroll
  for (int n = 0; n < 4; ++n) {
    z[n] = mfma16(tr2(a, n * 32, n * 32 + 512), b0, f32x4{0.f, 0.f, 0.f, 0.f});
    z[n] = mfma16(tr2(a, 4096 + n * 32, 4096 + n * 32 + 512), b1, z[n]);
  }
  return (r & 4) ? ((r & 2) ? z[3] : z[2]) : ((r & 2) ? z[1] : z[0]);
}
#define BAR() asm volatile("s_waitcnt lgkmcnt(0)\n\ts_barrier" ::: "memory")
#define FENCE() asm volatile("" ::: "memory")
#define VMW(N) do { __builtin_amdgcn_s_waitcnt(0x0F70 | ((N) & 15) | (((N) >> 4) << 14)); SCHED(); } while (0)

__device__ __forceinline__ unsigned rotr(unsigned x, unsigned n) { return __builtin_amdgcn_alignbit(x, x, n); }
__device__ __forceinline__ unsigned sum4(u32x4 w) { return w[0] + rotr(w[1], 8) + rotr(w[2], 16) + rotr(w[3], 24); }
__device__ __forceinline__ unsigned sum4_top(u32x4 w) {
  return (w[0] & 0xffffff00u) + __builtin_amdgcn_perm(0u, w[1], 0x0c030201u) + __builtin_amdgcn_perm(0u, w[2], 0x010c0302u) + __builtin_amdgcn_perm(0u, w[3], 0x02010c03u);
}
__device__ __forceinline__ u32x4 low_bytes(u32x4 w, unsigned d) {
  return u32x4{__builtin_amdgcn_perm(w[0], d, 0x07060500u), __builtin_amdgcn_perm(w[1], d, 0x07060503u), __builtin_amdgcn_perm(w[2], d, 0x07060502u), __builtin_amdgcn_perm(w[3], d, 0x07060501u)};
}
__device__ __forceinline__ u32x4 seal(f32x4 v, unsigned salt) { const u32x4 w = __builtin_bit_cast(u32x4, v); return low_bytes(w, salt - sum4_top(w)); }
__device__ __forceinline__ f32x4 vals(u32x4 w) { return __builtin_bit_cast(f32x4, w & 0xffffff00u); }
__device__ __forceinline__ f32x4 halves_sum(f32x4 v) {
  auto a = __builtin_amdgcn_permlane32_swap(__float_as_uint(v[0]), __float_as_uint(v[0]), false, false), b = __builtin_amdgcn_permlane32_swap(__float_as_uint(v[1]), __float_as_uint(v[1]), false, false);
  auto c = __builtin_amdgcn_permlane32_swap(__float_as_uint(v[2]), __float_as_uint(v[2]), false, false), d = __builtin_amdgcn_permlane32_swap(__float_as_uint(v[3]), __float_as_uint(v[3]), false, false);
  return f32x4{__uint_as_float(a[0]) + __uint_as_float(a[1]), __uint_as_float(b[0]) + __uint_as_float(b[1]), __uint_as_float(c[0]) + __uint_as_float(c[1]), __uint_as_float(d[0]) + __uint_as_float(d[1])};
}
#define SALT(g) ((unsigned)(g) * 0x9E3779B1u + 0x7F4A7C15u)
#define GIVE_UP(mask) do { --spin; mask = spin > 0 ? mask : 0u; asm volatile("" : "+v"(mask), "+v"(spin)); } while (0)
#define PIN(x) asm volatile("" : "+v"(x))

extern "C" __global__ void __launch_bounds__(512, 2)
fused_decode(const u16* a_x, const u16* a_k, const u16* a_v, const u16* a_wq, const u16* a_wo, const u16* a_w1, const u16* a_b1, const u16* a_w2, const u16* a_b2,
             u16* a_out, unsigned char* a_ws) {
  __shared__ __attribute__((aligned(16))) unsigned char smem[L_END];
  LAS unsigned char* const L = (LAS unsigned char*)smem;
#define ARGS() const CAS u64* kp_ = (const CAS u64*)__builtin_amdgcn_kernarg_segment_ptr(); asm volatile("" : "+s"(kp_)); \
  const u16* const x = (const u16*)kp_[0]; const u16* const Kc = (const u16*)kp_[1]; const u16* const Vc = (const u16*)kp_[2]; const u16* const wq = (const u16*)kp_[3]; \
  const u16* const wo = (const u16*)kp_[4]; const u16* const w1 = (const u16*)kp_[5]; const u16* const b1 = (const u16*)kp_[6]; const u16* const w2 = (const u16*)kp_[7]; \
  const u16* const b2 = (const u16*)kp_[8]; u16* const out = (u16*)kp_[9]; unsigned char* const ws = (unsigned char*)kp_[10]; \
  const auto rw = __builtin_amdgcn_make_buffer_rsrc(ws, 0, WS_END, RS3); \
  (void)x; (void)Kc; (void)Vc; (void)wq; (void)wo; (void)w1; (void)b1; (void)w2; (void)b2; (void)out; (void)rw
  const int lane = threadIdx.x & 63;
  int wave = __builtin_amdgcn_readfirstlane(threadIdx.x >> 6);
  const int xcd = blockIdx.x & 7, j = blockIdx.x >> 3, lw = xcd * 32 + j;
  const int g = lane >> 4, r = lane & 15;
  const unsigned box = WS_BOX + lw * BOX;
  const int pr = (xcd & 3) * 32 + j, ab = (pr >> 3) & 1, akv = pr >> 4, slot = (pr & 7) * 2 + (xcd >> 2);
  const bool mlp = j < 28;
  const int jm = mlp ? j : 0;
  float xres = 0.f, bias2 = 0.f;
  u32x4 zp = {0u, 0u, 0u, 0u};
  unsigned salt = 0;
  u64 gnext = 0;

  if (wave < 4) {
    ARGS();
    asm volatile("s_setprio 3" ::: "memory");
    SCHED();
    unsigned vq = (wave * 32 + (lane >> 3)) * 8192 + (lane & 7) * 16, v1 = (wave * 16 + (lane >> 3)) * 3584 + (lane & 7) * 16;
    unsigned vx = (wave & 1) * 1024 + lane * 16;
    unsigned qo = ((j & 3) * 128 * 4096 + xcd * 512 + (j >> 2) * 64) * 2, o1 = (xcd * 64 * 1792 + jm * 64) * 2, len1 = mlp ? 63u * 3584u + 128u : 0u;
    constexpr unsigned PSTR = 8192, WSTR = 2048;
    unsigned koff = wave * WSTR + (r >> 2) * 256 + g * 64 + (r & 3) * 16, voff = wave * WSTR + lane * 16, kvo = ((ab * 8 + akv) * SK + slot * 2048) * 256u;
    asm volatile("" : "+v"(vx), "+v"(vq), "+v"(v1), "+v"(koff), "+v"(voff), "+s"(qo), "+s"(o1), "+s"(len1), "+s"(kvo));
    {
      const u64 qb = (u64)wq + qo, b1_ = (u64)w1 + o1;
      const u32x4 rq = {(unsigned)qb, (unsigned)(qb >> 32) & 0xffffu, 127u * 8192u + 128u, (unsigned)RS3};
      const u32x4 r1 = {(unsigned)b1_, (unsigned)(b1_ >> 32) & 0xffffu, len1, (unsigned)RS3};
      const u32x4 rx = {(unsigned)(u64)x, (unsigned)((u64)x >> 32) & 0xffffu, 2048u, (unsigned)RS3};
      asm volatile("s_mov_b32 m0, %12\n\ts_nop 3\n\tbuffer_load_dwordx4 %10, %11, 0 offen lds\n\t"
                   "s_mov_b32 m0, %2\n\ts_nop 0\n\t"
                   "buffer_load_dwordx4 %0, %1, 0 offen nt lds\n\t"
                   "s_add_u32 m0, m0, 0x400\n\ts_nop 0\n\tbuffer_load_dwordx4 %0, %1, %3 offen nt lds\n\t"
                   "s_add_u32 m0, m0, 0x400\n\ts_nop 0\n\tbuffer_load_dwordx4 %0, %1, %4 offen nt lds\n\t"
                   "s_add_u32 m0, m0, 0x400\n\ts_nop 0\n\tbuffer_load_dwordx4 %0, %1, %5 offen nt lds\n\t"
                   "s_mov_b32 m0, %8\n\ts_nop 0\n\tbuffer_load_dwordx4 %6, %7, 0 offen nt lds\n\t"
                   "s_add_u32 m0, m0, 0x400\n\ts_nop 0\n\tbuffer_load_dwordx4 %6, %7, %9 offen nt lds"
                   :: "v"(vq), "s"(rq), "s"((unsigned)(size_t)(L + L_WQ) + wave * 4096), "s"(0x10000u), "s"(0x20000u), "s"(0x30000u),
                      "v"(v1), "s"(r1), "s"((unsigned)(size_t)(L + L_W1) + wave * 2048), "s"(8u * 3584u),
                      "v"(vx), "s"(rx), "s"((unsigned)(size_t)(L + L_XN) + wave * 1024) : "memory", "scc");
    }
    __builtin_amdgcn_s_setprio(0);
    const CAS u64* gp = (const CAS u64*)(ws + WS_G);
    asm volatile("" : "+s"(gp));
    const u64 gprev = *gp;
    const auto rk = __builtin_amdgcn_make_buffer_rsrc((char*)Kc + kvo, 0, 2048 * 256, RS3);
    const auto rv = __builtin_amdgcn_make_buffer_rsrc((char*)Vc + kvo, 0, 2048 * 256, RS3);
    u32x4 tb[4][8];
#define LOAD(B, I, RS, VO, T) do { tb[B][I] = __builtin_amdgcn_raw_buffer_load_b128(RS, (VO) + ((I) & 1) * 1024, (unsigned)(T) * 32768u + ((I) >> 1) * PSTR, AUX_NT); SCHED(); } while (0)
#define PRE(H) do { if ((H) == P0) asm volatile("s_waitcnt vmcnt(%0)\n\ts_barrier" :: "n"(6 + P0) : "memory");   \
    if ((H) > P0) VMW(PW - 1); \
    if ((H) == PW - 1) { FENCE(); *(volatile LAS int*)(L + L_F1 + wave * 4) = 1; SCHED(); }     \
    LOAD((H) >> 3, (H) & 7, rk, koff, (H) >> 3); } while (0)
#define PRE2(H) do { if ((H) < LK) PRE(H); } while (0)
#define PRE3(H) do { if ((H) >= LK) PRE(H); } while (0)
#define X8(M, H) do { M(H); M((H) + 1); M((H) + 2); M((H) + 3); M((H) + 4); M((H) + 5); M((H) + 6); M((H) + 7); } while (0)
    SCHED();
    X8(PRE, 0); X8(PRE, 8);
    { u64 g_ = gprev; asm volatile("" : "+s"(g_)); salt = SALT(g_ + 1); }
    X8(PRE2, 16); X8(PRE2, 24);
    const unsigned qsrc = WS_QG + akv * 16384 + (lane >> 5) * 4096 + (wave * 8 + ((lane >> 2) & 7)) * 128 + ab * 64 + (lane & 3) * 16;
    u32x4 qv[2];
    {
      const u32x4 rwq = {(unsigned)(u64)ws, (unsigned)((u64)ws >> 32) & 0xffffu, WS_END, (unsigned)RS3};
      asm volatile("s_waitcnt vmcnt(%5)\n\tbuffer_load_dwordx4 %0, %2, %3, 0 offen sc1\n\tbuffer_load_dwordx4 %1, %2, %3, %4 offen sc1"
                   : "=&v"(qv[0]), "=&v"(qv[1]) : "v"(qsrc), "s"(rwq), "s"(8192u), "n"(PW - 2) : "memory");
    }
    X8(PRE3, 16); X8(PRE3, 24);
    {
      const u64 b2_ = (u64)(w2 + jm * 64 * 512 + xcd * 64), bo = (u64)(wo + (xcd * 512 + (j >> 3) * 128) * 512 + (j & 7) * 64);
      const u32x4 r2 = {(unsigned)b2_, (unsigned)(b2_ >> 32) & 0xffffu, mlp ? 63u * 1024u + 128u : 0u, (unsigned)RS3};
      const u32x4 ro = {(unsigned)bo, (unsigned)(bo >> 32) & 0xffffu, 127u * 1024u + 128u, (unsigned)RS3};
#define DMA2(V, R, LB, S0, S1) asm volatile("s_waitcnt vmcnt(%5)\n\ts_mov_b32 m0, %2\n\ts_nop 0\n\tbuffer_load_dwordx4 %0, %1, %3 offen nt lds\n\t" \
                     "s_add_u32 m0, m0, 0x400\n\ts_waitcnt vmcnt(%5)\n\tbuffer_load_dwordx4 %0, %1, %4 offen nt lds" :: "v"(V), "s"(R), "s"(LB), "s"(S0), "s"(S1), "n"(PW - 1) : "memory", "scc")
      const unsigned v2 = (wave * 16 + (lane >> 3)) * 1024 + (lane & 7) * 16, vo = (lane >> 3) * 8192 + wave * 2048 + (lane & 7) * 16;
      DMA2(v2, r2, (unsigned)(size_t)(L + L_W2) + wave * 2048, 0u, 8192u);
      DMA2(vo, ro, (unsigned)(size_t)(L + L_WO) + wave * 4096, 0u, 65536u);
      DMA2(vo, ro, (unsigned)(size_t)(L + L_WO) + wave * 4096 + 2048, 1024u, 65536u + 1024u);
    }
    asm volatile("s_waitcnt vmcnt(%2)" : "+v"(qv[0]), "+v"(qv[1]) : "n"(32 - LK + 6));
    {
#define QBAD() (__builtin_amdgcn_ballot_w64(((sum4(qv[0]) ^ salt) | (sum4(qv[1]) ^ salt)) != 0) != 0)
      if (__builtin_expect(QBAD(), 0)) {
#pragma unroll 1
        for (unsigned spin = 0; spin < SPIN_CAP; ++spin) {
          FENCE();
          qv[0] = __builtin_amdgcn_raw_buffer_load_b128(rw, qsrc, 0, AUX_SC1); qv[1] = __builtin_amdgcn_raw_buffer_load_b128(rw, qsrc + 8192, 0, AUX_SC1);
          if (!QBAD()) break;
        }
      }
      const f32x4 q4 = halves_sum(vals(qv[0]) + vals(qv[1]));
      *(LAS u32x2*)(L + L_Q + wave * 256 + (lane & 31) * 8) = u32x2{cvtpk(q4[0], q4[1]), cvtpk(q4[2], q4[3])};
    }
    BAR();
    const int head = (r & 3) ^ ((r & 4) ? 3 : 0);
    const u32x4 qd = *(LAS u32x4*)(L + L_Q + head * 256 + g * 64 + (r >> 2) * 16);
    LAS unsigned char* sr = L + L_S + wave * 8192 + g * 128 + head * 32;
    LAS unsigned char* sw = sr + (r & 4) * 4 + (r >> 3) * 4;
    float m = -3.0e38f, y[4];
#define QP(B, P) do { const f32x4 z4 = {0.f, 0.f, 0.f, 0.f}, da = mfma16(tb[B][2 * (P)], qd, z4), db = mfma16(tb[B][2 * (P) + 1], qd, z4); \
    const float a0 = (r & 4) ? da[1] : da[0], a1 = (r & 4) ? da[3] : da[2], b0 = (r & 4) ? db[1] : db[0], b1 = (r & 4) ? db[3] : db[2]; \
    y[P] = ((r & 8) ? b1 : a0) + dpp<ROR8>((r & 8) ? a1 : b0); } while (0)
#define QE(T) do { const float z0 = ((r & 4) ? y[2] : y[0]) + dpp<HMIR>((r & 4) ? y[0] : y[2]), z1 = ((r & 4) ? y[3] : y[1]) + dpp<HMIR>((r & 4) ? y[1] : y[3]); \
    m = fmaxf(m, fmaxf(z0, z1)); *(LAS float*)(sw + (T) * 512) = z0; *(LAS float*)(sw + (T) * 512 + 8) = z1; } while (0)
#define QS(B, T) do { QP(B, 0); QP(B, 1); QP(B, 2); QP(B, 3); QE(T); SCHED(); } while (0)
#define CATCH(I) do { if ((I) < AH) LOAD(0, I, rk, koff, 4); } while (0)
    QS(0, 0); CATCH(0); CATCH(1); CATCH(2); CATCH(3); CATCH(4); CATCH(5); CATCH(6); CATCH(7);
    QS(1, 1); QS(2, 2); QS(3, 3);
    VMW(AH);
    *(volatile LAS int*)(L + L_PRG + wave * 4) = 1;
    LAS unsigned char* la = L + L_A + (wave * 4 + r) * A_STR + g * 16;
    PIN(la);
    int lag = 0;
#define WAITK() do { asm volatile("s_cmp_gt_i32 %0, 0\n\ts_cbranch_scc1 .Lw%=\n\ts_waitcnt vmcnt(%1)\n.Lw%=:\n\ts_waitcnt vmcnt(%2)" :: "s"(lag), "n"(WK - 1), "n"(WK) : "scc"); SCHED(); } while (0)
#define CTL0() const u64 now64_ = __builtin_amdgcn_s_memrealtime(); const u32x2 lv_ = *(volatile LAS u32x2*)(L + L_LINE); SCHED()
#define CTL1(N) do { asm volatile("" :: "s"(now64_)); const unsigned now_ = (unsigned)now64_; lag = __builtin_amdgcn_readfirstlane((now_ - lv_[1] - (N) * CB) & lv_[0]); \
    if (wave == 0) *(volatile LAS u32x2*)(L + L_CK) = u32x2{(unsigned)(N), now_}; SCHED(); } while (0)
#define STEP(H, RS, VO, RN, VN, TN) do { WAITK(); \
    if ((H) + AH < 16) LOAD(((H) + AH) >> 3, ((H) + AH) & 7, RS, VO, t + (((H) + AH) >> 3)); else LOAD(0, ((H) + AH) & 7, RN, VN, TN); } while (0)
#define STEP2(H, ...) do { STEP(H, __VA_ARGS__); STEP((H) + 1, __VA_ARGS__); } while (0)
#define KT(B, ...) do { CTL0(); STEP2(8 * (B), __VA_ARGS__); CTL1(t + (B)); QP(B, 0); SCHED(); STEP2(8 * (B) + 2, __VA_ARGS__); QP(B, 1); SCHED(); \
    STEP2(8 * (B) + 4, __VA_ARGS__); QP(B, 2); SCHED(); STEP2(8 * (B) + 6, __VA_ARGS__); QP(B, 3); QE(t + (B)); SCHED(); } while (0)
#pragma unroll 1
    for (int t = 4; t < 16; t += 2) {
      const bool last = t == 14;
      const auto rn = __builtin_amdgcn_make_buffer_rsrc((last ? (char*)Vc : (char*)Kc) + kvo, 0, 2048 * 256, RS3);
      const unsigned vn = last ? voff : koff, tn = (t + 2) & 15;
      KT(0, rk, koff, rn, vn, tn); KT(1, rk, koff, rn, vn, tn);
    }
    m = fmaxf(m, dpp<HMIR>(m)); m = fmaxf(m, dpp<ROR8>(m));
    const float mc = xrow_max(m) * C2;
    float l = 0.f;
    f32x4 acc[8];
#pragma unroll
    for (int e = 0; e < 8; ++e) acc[e] = f32x4{0.f, 0.f, 0.f, 0.f};
    u32x4 pf, vt[8];
#define SP(T) do { const f32x4 s0 = *(LAS f32x4*)(sr + (T) * 512), s1 = *(LAS f32x4*)(sr + (T) * 512 + 16); float p[8]; \
    _Pragma("unroll") for (int i = 0; i < 4; ++i) { p[i] = __builtin_amdgcn_exp2f(fmaf(s0[i], C2, -mc)); p[4 + i] = __builtin_amdgcn_exp2f(fmaf(s1[i], C2, -mc)); } \
    l += ((p[0] + p[1]) + (p[2] + p[3])) + ((p[4] + p[5]) + (p[6] + p[7])); \
    pf = u32x4{cvtpk(p[0], p[1]), cvtpk(p[2], p[3]), cvtpk(p[4], p[5]), cvtpk(p[6], p[7])}; SCHED(); } while (0)
#define VP(B, P) do { _Pragma("unroll") for (int w = 0; w < 4; ++w) { vt[2 * w][P] = __builtin_amdgcn_perm(tb[B][2 * (P) + 1][w], tb[B][2 * (P)][w], 0x05040100u); \
      vt[2 * w + 1][P] = __builtin_amdgcn_perm(tb[B][2 * (P) + 1][w], tb[B][2 * (P)][w], 0x07060302u); } } while (0)
#define VE() do { _Pragma("unroll") for (int e = 0; e < 8; ++e) acc[e] = mfma16(vt[e], pf, acc[e]); SCHED(); } while (0)
#define VT(B, ...) do { CTL0(); STEP2(8 * (B), __VA_ARGS__); CTL1(16 + t + (B)); VP(B, 0); SP(t + (B)); STEP2(8 * (B) + 2, __VA_ARGS__); VP(B, 1); SCHED(); \
    STEP2(8 * (B) + 4, __VA_ARGS__); VP(B, 2); SCHED(); STEP2(8 * (B) + 6, __VA_ARGS__); VP(B, 3); VE(); } while (0)
#pragma unroll 1
    for (int t = 0; t < TE; t += 2) { VT(0, rv, voff, rv, voff, t + 2); VT(1, rv, voff, rv, voff, t + 2); }
#define EL(N) do { if ((N) < 8 * (16 - TE)) { if ((N) >= WE) VMW(WE - 1); LOAD((N) >> 3, (N) & 7, rv, voff, TE + ((N) >> 3)); } } while (0)
#define EP(N) do { if ((N) >= AH && (N) < WE) EL(N); } while (0)
    X8(EP, 0); X8(EP, 8); X8(EP, 16); X8(EP, 24);
#define ET(B) do { if (TE + (B) < 16) { SP(TE + (B)); EL(WE + 8 * (B)); EL(WE + 8 * (B) + 1); VP(B, 0); SCHED(); EL(WE + 8 * (B) + 2); EL(WE + 8 * (B) + 3); VP(B, 1); SCHED(); \
      EL(WE + 8 * (B) + 4); EL(WE + 8 * (B) + 5); VP(B, 2); SCHED(); EL(WE + 8 * (B) + 6); EL(WE + 8 * (B) + 7); VP(B, 3); VE(); } } while (0)
    ET(0); ET(1); ET(2); ET(3);
    l = xrow_sum(l);
    if (r < 4) {
#pragma unroll
      for (int e = 0; e < 8; ++e) *(LAS f32x4*)(la + e * 64) = acc[e];
      *(LAS float*)(L + L_M + r * 16 + wave * 4) = mc; *(LAS float*)(L + L_L + r * 16 + wave * 4) = l;
    }
    BAR();
  }
  asm volatile("" : "+s"(wave));
  if (wave >= 4) {
    __builtin_amdgcn_s_setprio(2);
    ARGS();
    const int n = wave - 4;
    unsigned seen = 0;
    if (n == 2) seen = __builtin_amdgcn_raw_buffer_load_b32(rw, lane < 24 ? box + lane * 128 : lane < 28 ? WS_QG + xcd * 16384 + (j & 3) * 4096 + ((j >> 2) * 4 + (lane & 3)) * 128
                                                              : (lane < 32 && mlp) ? WS_HG + (j * 8 + xcd) * 512 + (lane & 3) * 128 : (lane == 32 && blockIdx.x < 16) ? WS_TS + blockIdx.x * 128 : OOB, 0, 0);
    gnext = *(const CAS u64*)(ws + WS_G) + 1;
    *(volatile LAS int*)(L + L_PRG + n * 4) = 0; *(volatile LAS int*)(L + L_F1 + n * 4) = 0; *(volatile LAS int*)(L + L_F1 + 16 + n * 4) = 0; *(volatile LAS int*)(L + L_CK + n * 4) = 0;
    unsigned qdst = r < 2 ? WS_QG + xcd * 16384 + (j & 3) * 4096 + ((j >> 2) * 4 + n) * 128 + r * 64 + g * 16 : OOB;
    unsigned hdst = (r < 2 && mlp) ? WS_HG + (j * 8 + xcd) * 512 + n * 128 + r * 64 + g * 16 : OOB;
    PIN(qdst); PIN(hdst);
    BAR();
    u32x4 xr = *(LAS u32x4*)(L + L_XN + n * 1024 + lane * 16);
#pragma unroll 1
    for (int it = n >> 1; it >= 0; --it) xr = ln8(xr);
    *(LAS u32x4*)(L + L_XN + n * 1024 + lane * 16) = xr;
    salt = SALT(gnext);
    asm volatile("s_waitcnt lgkmcnt(0)" : "+s"(salt), "+s"(gnext) :: "memory");
    *(volatile LAS int*)(L + L_F1 + 16 + n * 4) = 1;
#pragma unroll 1
    for (unsigned spin = 0; spin < 16 * SPIN_CAP; ++spin) {
      const u32x4 p = *(volatile LAS u32x4*)(L + L_F1) & *(volatile LAS u32x4*)(L + L_F1 + 16);
      if ((p[0] & p[1] & p[2] & p[3]) != 0) break;
    }
    FENCE();
    const int ao = (8 * g + (r >> 2)) * 128 + (r & 3) * 8 + n * 32;
    LAS unsigned char* const bp = L + L_XN + (r & 1) * 1024 + g * 16;
    f32x4 qv = {0.f, 0.f, 0.f, 0.f}, hv = {0.f, 0.f, 0.f, 0.f};
#pragma unroll
    for (int k = 0; k < 4; ++k) qv = mfma16(tr2(L + L_WQ + ao, k * 4096, k * 4096 + 512), *(LAS u32x4*)(bp + (j & 3) * 256 + k * 64), qv);
#pragma unroll
    for (int k = 0; k < 2; ++k) hv = mfma16(tr2(L + L_W1 + ao, k * 4096, k * 4096 + 512), *(LAS u32x4*)(bp + 2048 + xcd * 128 + k * 64), hv);
    __builtin_amdgcn_raw_buffer_store_b128(seal(qv, salt), rw, qdst, 0, AUX_SC1);
    __builtin_amdgcn_raw_buffer_store_b128(seal(hv, salt), rw, hdst, 0, AUX_SC1);
    if (n == 3) {
      u64 pc; unsigned back, len;
      asm volatile("s_getpc_b64 %0\n.Lpc%=:\n\ts_mov_b32 %1, .Lpc%=-fused_decode\n\ts_mov_b32 %2, .Lfd_last-fused_decode" : "=s"(pc), "=s"(back), "=s"(len));
      seen = __builtin_amdgcn_raw_buffer_load_b128(__builtin_amdgcn_make_buffer_rsrc((void*)(pc - back), 0, len, RS3), lane < 40 ? j * 640 + lane * 16 : OOB, 0, 0)[0];
    }
    asm volatile("s_barrier" :: "v"(seen) : "memory");
  }
  asm volatile("" : "+s"(wave));
  if (wave == 6) {
    ARGS();
    __builtin_amdgcn_s_setprio(0);
#pragma unroll 1
    for (unsigned spin = 0; spin < SPIN_CAP; ++spin) {
      const u32x4 p = *(volatile LAS u32x4*)(L + L_PRG);
      if (min(min(p[0], p[1]), min(p[2], p[3])) != 0) break;
      __builtin_amdgcn_s_sleep(16);
    }
    SCHED();
    __builtin_amdgcn_s_setprio(3);
    const int hcol = ((lane >> 3) & 3) * 16 + (lane & 3) * 4;
    const u32x2 bb = *(const GAS u32x2*)(b1 + jm * 64 + hcol);
    const int ocol = xcd * 64 + (j & 15) * 4 + g;
    xres = blo(((const GAS u16*)x)[(lane & 1) * 512 + ocol]);
    bias2 = blo(((const GAS u16*)b2)[ocol]);
    u32x4 v[4];
#pragma unroll 1
    for (unsigned spin = 0; spin < SPIN_CAP; ++spin) {
      FENCE();
#pragma unroll
      for (int k = 0; k < 4; ++k) v[k] = __builtin_amdgcn_raw_buffer_load_b128(rw, WS_HG + jm * 4096 + k * 1024 + lane * 16, 0, AUX_SC1);
      if (__builtin_amdgcn_ballot_w64(((sum4(v[0]) ^ salt) | (sum4(v[1]) ^ salt) | (sum4(v[2]) ^ salt) | (sum4(v[3]) ^ salt)) != 0) == 0) break;
      __builtin_amdgcn_s_sleep(16);
    }
    SCHED();
    __builtin_amdgcn_s_setprio(0);
    const f32x4 hs = halves_sum((vals(v[0]) + vals(v[1])) + (vals(v[2]) + vals(v[3])));
    float hh[4];
#pragma unroll
    for (int i = 0; i < 4; ++i) {
      const float z = rbf(rbf(hs[i]) + ((i & 1) ? bhi(bb[i >> 1]) : blo(bb[i >> 1])));
      const float u = 0.7978845608028654f * (z + 0.044715f * z * z * z);
      hh[i] = z * __builtin_amdgcn_rcpf(1.f + __builtin_amdgcn_exp2f(-2.f * LOG2E * u));
    }
    *(LAS u32x2*)(L + L_H + ((lane >> 2) & 1) * 128 + hcol * 2) = u32x2{cvtpk(hh[0], hh[1]), cvtpk(hh[2], hh[3])};
    zp = seal(gemv64(L + L_W2, L + L_H + (r & 1) * 128, g, r), salt);
    PIN(zp); PIN(xres); PIN(bias2);
  }
  asm volatile("" : "+s"(wave));
  ARGS();
  if (wave == 7) {
    const bool sample = (j & 7) == ((xcd + 3 * (j >> 3)) & 7);
#pragma unroll 1
    for (int n = 4, k = 0; n + CL < 16 + TE; n += CS, ++k) {
      u32x2 ck;
#pragma unroll 1
      for (unsigned spin = 0; spin < SPIN_CAP; ++spin) { ck = *(volatile LAS u32x2*)(L + L_CK); if ((int)ck[0] >= n) break; __builtin_amdgcn_s_sleep(4); }
      const unsigned tk = ck[1] - (ck[0] - n) * CB;
      __builtin_amdgcn_raw_buffer_store_b128(seal(__builtin_bit_cast(f32x4, u32x4{tk << 8, 0u, 0u, 0u}), salt), rw, (sample && lane == 0) ? WS_TS + k * 512 + ((j >> 3) * 8 + xcd) * 16 : OOB, 0, AUX_SC1);
#pragma unroll 1
      for (unsigned spin = 0; spin < SPIN_CAP; ++spin) { if (*(volatile LAS int*)(L + L_CK) >= n + CL) break; __builtin_amdgcn_s_sleep(4); }
      const u32x4 v = __builtin_amdgcn_raw_buffer_load_b128(rw, lane < 32 ? WS_TS + k * 512 + lane * 16 : OOB, 0, AUX_SC1);
      const bool ok = lane < 32 && sum4(v) == salt;
      const int cnt = __builtin_popcountll(__builtin_amdgcn_ballot_w64(ok));
      const float mean = wave_sum(ok ? (float)((int)((v[0] & 0xffffff00u) - (tk << 8)) >> 8) : 0.f) * __builtin_amdgcn_rcpf((float)cnt);
      if (cnt >= 16) *(volatile LAS u32x2*)(L + L_LINE) = u32x2{~0u, tk + (int)mean - n * CB};
    }
  }
  if (wave >= 4) {
    unsigned dst1 = wave == 7 ? OOB : wave == 6 ? ((r < 8 && mlp) ? box + BX_ZB + (r >> 1) * 128 + (r & 1) * 64 + g * 16 : OOB)
                                                : WS_BOX + (akv * 32 + slot * 2 + ab) * BOX + BX_PA + ((wave & 1) * 64 + lane) * 16;
    const int hh = (wave & 1) * 2 + (lane >> 5);
    LAS unsigned char *lm = L + L_M + hh * 16, *la = L + L_A + hh * A_STR + (lane & 31) * 16;
    PIN(dst1); PIN(lm); PIN(la);
    asm volatile("" :: "s"(ws));
    BAR();
    if (wave < 6) {
      const f32x4 mm = *(LAS f32x4*)lm, ll = *(LAS f32x4*)(lm + (L_L - L_M));
      f32x4 a[4];
#pragma unroll
      for (int w = 0; w < 4; ++w) a[w] = *(LAS f32x4*)(la + w * 4 * A_STR);
      SCHED();
      const float M = max3(max3(mm[0], mm[1], mm[2]), mm[3], mm[3]);
      float wt[4];
#pragma unroll
      for (int w = 0; w < 4; ++w) wt[w] = __builtin_amdgcn_exp2f(mm[w] - M);
      const float Ls = fmaf(ll[3], wt[3], fmaf(ll[2], wt[2], fmaf(ll[1], wt[1], ll[0] * wt[0])));
      f32x4 o = a[0] * wt[0];
#pragma unroll
      for (int w = 1; w < 4; ++w) o = __builtin_elementwise_fma(a[w], f32x4{wt[w], wt[w], wt[w], wt[w]}, o);
      o *= __builtin_amdgcn_rcpf(Ls);
      const unsigned lse = __float_as_uint(M + __builtin_amdgcn_logf(Ls));
      const u32x4 w = __builtin_bit_cast(u32x4, o);
      const unsigned sw = sum4_top(w), so = (unsigned)__builtin_amdgcn_mov_dpp((int)sw, XOR1, 0xf, 0xf, true);
      zp = low_bytes(w, (lane & 1) ? lse : salt - sw - rotr(so + lse, 5));
    }
    __builtin_amdgcn_raw_buffer_store_b128(zp, rw, dst1, 0, AUX_SC1);
  }
  unsigned psrc = WS_BOX + (xcd * 32 + (lane >> 3) * 2 + (wave >> 2)) * BOX + BX_PA + (j >> 3) * 512 + (wave & 3) * 128 + (lane & 7) * 16;
  unsigned dst2 = r < 2 ? box + BX_SB + (wave & 3) * 128 + r * 64 + g * 16 : OOB;
  LAS unsigned char* lo = L + L_O + (wave >> 2) * 256 + ((wave & 3) * 32 + (lane & 7) * 4 + g) * 2;
  LAS unsigned char* lb = L + L_O + (r & 1) * 256 + g * 16;
  PIN(dst2); PIN(lo); PIN(lb);
  LAS unsigned char* const aw = L + L_WO + (8 * g + (r >> 2)) * 128 + (r & 3) * 8 + (wave & 3) * 32;
  u32x4 wa[4];
#pragma unroll
  for (int k = 0; k < 4; ++k) wa[k] = tr2(aw, k * 4096, k * 4096 + 512);
  {
    u32x4 v0, v1;
    int spin = SPIN_CAP;
    unsigned even = (lane & 1) - 1u;
    PIN(even);
    __builtin_amdgcn_s_sleep(PD);
#pragma unroll 1
    do {
      FENCE();
      v0 = __builtin_amdgcn_raw_buffer_load_b128(rw, psrc, 0, AUX_SC1);
      v1 = __builtin_amdgcn_raw_buffer_load_b128(rw, psrc + 16 * BOX, 0, AUX_SC1);
      const unsigned s0 = sum4(v0), s1 = sum4(v1);
      const unsigned c0 = s0 + rotr((unsigned)__builtin_amdgcn_mov_dpp((int)s0, XOR1, 0xf, 0xf, true), 5), c1 = s1 + rotr((unsigned)__builtin_amdgcn_mov_dpp((int)s1, XOR1, 0xf, 0xf, true), 5);
      if (__builtin_amdgcn_ballot_w64((((c0 ^ salt) | (c1 ^ salt)) & even) != 0) == 0) break;
      __builtin_amdgcn_s_sleep(PSL);
      GIVE_UP(even);
    } while (true);
#define LOWB(v) __uint_as_float(__builtin_amdgcn_perm(__builtin_amdgcn_perm(v[1], v[2], 0x04000000u), __builtin_amdgcn_perm(v[3], v[0], 0x00000400u), 0x07060100u))
    const float e0 = dpp<ODD>(LOWB(v0)), e1 = dpp<ODD>(LOWB(v1));
    const float M = xrow_max(max3(max3(e0, e1, dpp<ROR8>(e0)), dpp<ROR8>(e1), e0));
    const float w0 = __builtin_amdgcn_exp2f(e0 - M), w1_ = __builtin_amdgcn_exp2f(e1 - M);
    float Ws = w0 + w1_; Ws += dpp<ROR8>(Ws); Ws = xrow_sum(Ws);
    float o = rows_reduce4(__builtin_elementwise_fma(vals(v1), f32x4{w1_, w1_, w1_, w1_}, vals(v0) * w0));
    o += dpp<ROR8>(o);
    o *= __builtin_amdgcn_rcpf(Ws);
    *(LAS u16*)lo = (u16)cvtpk(o, o);
  }
  BAR();
  if (wave < 4) {
    u32x4 ob[4];
#pragma unroll
    for (int k = 0; k < 4; ++k) ob[k] = *(LAS u32x4*)(lb + k * 64);
    SCHED();
    const f32x4 z4 = {0.f, 0.f, 0.f, 0.f};
    const f32x4 s = mfma16(wa[1], ob[1], mfma16(wa[0], ob[0], z4)) + mfma16(wa[3], ob[3], mfma16(wa[2], ob[2], z4));
    __builtin_amdgcn_raw_buffer_store_b128(seal(s, salt), rw, dst2, 0, AUX_SC1);
  }
  if (wave == 6 && j < 16) {
    const unsigned in = (j >> 2) * 128 + (lane & 1) * 64 + (j & 3) * 16;
    const unsigned zsrc = lane < 56 ? WS_BOX + (xcd * 32 + (lane >> 1)) * BOX + BX_ZB + in : OOB;
    const unsigned ssrc = WS_BOX + ((lane >> 3) * 32 + ((lane >> 1) & 3) * 8 + xcd) * BOX + BX_SB + in;
    u32x4 v;
    int spin = SPIN_CAP;
    unsigned need = lane < 56 ? ~0u : 0u;
#pragma unroll 1
    do {
      FENCE();
      v = __builtin_amdgcn_raw_buffer_load_b128(rw, zsrc, 0, AUX_SC1);
      if (__builtin_amdgcn_ballot_w64(((sum4(v) ^ salt) & need) != 0) == 0) break;
      GIVE_UP(need);
    } while (true);
    float z = rows_reduce4(vals(v));
    z += dpp<XOR2>(z); z += dpp<ROR4>(z); z += dpp<ROR8>(z);
    z = rbf(rbf(z) + bias2);
    float gate = rbf(z * __builtin_amdgcn_rcpf(1.f + __builtin_amdgcn_exp2f(-LOG2E * z)));
    unsigned odst = r < 2 ? (r * 512 + xcd * 64 + j * 4 + g) * 2 : OOB;
    PIN(gate); PIN(odst);
    spin = SPIN_CAP; need = ~0u;
#pragma unroll 1
    do {
      FENCE();
      v = __builtin_amdgcn_raw_buffer_load_b128(rw, ssrc, 0, AUX_SC1);
      if (__builtin_amdgcn_ballot_w64(((sum4(v) ^ salt) & need) != 0) == 0) break;
      GIVE_UP(need);
    } while (true);
    float s = rows_reduce4(vals(v));
    s += dpp<XOR2>(s); s += dpp<ROR4>(s); s += dpp<ROR8>(s);
    const float res = xres + rbf(rbf(s) * gate);
    const auto rout = __builtin_amdgcn_make_buffer_rsrc(out, 0, 2048, RS3);
    __builtin_amdgcn_raw_buffer_store_b16((u16)cvtpk(res, res), rout, odst, 0, 0);
    __builtin_amdgcn_raw_buffer_store_b64(u32x2{(unsigned)gnext, (unsigned)(gnext >> 32)}, rw, (blockIdx.x == 0 && lane == 0) ? WS_G : OOB, 0, AUX_SC1);
    asm volatile(".Lfd_last:");
  }
}

extern "C" void kernel_launch(void* const* d_in, const int* in_sizes, int n_in, void* d_out, int out_size, void* d_ws, size_t ws_size, hipStream_t stream) {
  fused_decode<<<256, 512, 0, stream>>>((const u16*)d_in[0], (const u16*)d_in[1], (const u16*)d_in[2], (const u16*)d_in[3], (const u16*)d_in[4],
                                        (const u16*)d_in[6], (const u16*)d_in[7], (const u16*)d_in[8], (const u16*)d_in[9], (u16*)d_out, (unsigned char*)d_ws);
}
```

```cpp
#include <hip/hip_runtime.h>
#include <stdint.h>

typedef unsigned short u16;
typedef unsigned long long u64;
typedef unsigned u32x2 __attribute__((ext_vector_type(2)));
typedef unsigned u32x4 __attribute__((ext_vector_type(4)));
typedef float f32x2 __attribute__((ext_vector_type(2)));
typedef float f32x4 __attribute__((ext_vector_type(4)));
typedef short s16x4 __attribute__((ext_vector_type(4)));
typedef short bf16x8 __attribute__((ext_vector_type(8)));
typedef __bf16 bf16x2_t __attribute__((ext_vector_type(2)));
#define GAS __attribute__((address_space(1)))
#define LAS __attribute__((address_space(3)))
#define CAS __attribute__((address_space(4)))

#define WK 5
#define WE 8
#define TE 14
#define PW 10
#define P0 1
#define LK 28
#define CB 112
#define CS 12
#define CL 2
#define PD 16
#define PSL 2
constexpr int AH = WK + 1;
static_assert((16 + TE - CL - 5) / CS < 4 && AH <= 8 && WE >= 8 && (TE == 12 || TE == 14) && P0 < PW - 1 && PW <= 16 && LK >= 16 && LK <= 32, "");

constexpr int SK = 32768;
constexpr int AUX_SC1 = 16, AUX_NT = 2, RS3 = 0x00020000;
constexpr unsigned WS_G = 0, WS_QG = 4096, WS_HG = WS_QG + 8 * 16384, WS_TS = WS_HG + 224 * 512, WS_BOX = WS_TS + 4 * 512;
constexpr unsigned BOX = 3072 + 128, BX_PA = 0, BX_SB = 2048, BX_ZB = 2560, WS_END = WS_BOX + 256 * BOX;
constexpr int L_WQ = 0, L_WO = 16384, L_W1 = 32768, L_W2 = 40960, L_XN = 49152, L_Y = L_XN + 2048, L_Q = 53248, L_H = 54272, L_O = 54528,
              L_M = 55040, L_L = 55104, L_PRG = 55168, L_F1 = 55184, L_CK = 55216, L_LINE = 55224, L_A = 55296, A_STR = 528, L_S = L_A + 16 * A_STR, L_END = L_S + 32768;
constexpr unsigned OOB = 0x80000000u;
constexpr unsigned SPIN_CAP = 1u << 19;
constexpr float LOG2E = 1.4426950408889634f, C2 = 0.08838834764831845f * LOG2E;

#define SCHED() __builtin_amdgcn_sched_barrier(0)

__device__ __forceinline__ f32x4 mfma16(u32x4 a, u32x4 b, f32x4 c) {
  return __builtin_amdgcn_mfma_f32_16x16x32_bf16(__builtin_bit_cast(bf16x8, a), __builtin_bit_cast(bf16x8, b), c, 0, 0, 0);
}
__device__ __forceinline__ unsigned cvtpk(float lo, float hi) { return __builtin_bit_cast(unsigned, __builtin_convertvector(f32x2{lo, hi}, bf16x2_t)); }
__device__ __forceinline__ float blo(unsigned u) { return __uint_as_float(u << 16); }
__device__ __forceinline__ float bhi(unsigned u) { return __uint_as_float(u & 0xffff0000u); }
__device__ __forceinline__ float rbf(float x) { return blo(cvtpk(x, x)); }
template <int CTRL> __device__ __forceinline__ float dpp(float x) {
  return __builtin_bit_cast(float, __builtin_amdgcn_mov_dpp(__builtin_bit_cast(int, x), CTRL, 0xf, 0xf, true));
}
constexpr int XOR1 = 0xB1, XOR2 = 0x4E, ROR4 = 0x124, ROR8 = 0x128, HMIR = 0x141, ODD = 0xF5;
__device__ __forceinline__ float max3(float a, float b, float c) { float d; asm("v_max3_f32 %0, %1, %2, %3" : "=v"(d) : "v"(a), "v"(b), "v"(c)); return d; }
__device__ __forceinline__ float xrow_max(float x) {
  auto s = __builtin_amdgcn_permlane16_swap(__float_as_uint(x), __float_as_uint(x), false, false);
  x = max3(__uint_as_float(s[0]), __uint_as_float(s[1]), __uint_as_float(s[1]));
  auto t = __builtin_amdgcn_permlane32_swap(__float_as_uint(x), __float_as_uint(x), false, false);
  return max3(__uint_as_float(t[0]), __uint_as_float(t[1]), __uint_as_float(t[1]));
}
__device__ __forceinline__ float xrow_sum(float x) {
  auto s = __builtin_amdgcn_permlane16_swap(__float_as_uint(x), __float_as_uint(x), false, false);
  x = __uint_as_float(s[0]) + __uint_as_float(s[1]);
  auto t = __builtin_amdgcn_permlane32_swap(__float_as_uint(x), __float_as_uint(x), false, false);
  return __uint_as_float(t[0]) + __uint_as_float(t[1]);
}
__device__ __forceinline__ float wave_sum(float x) {
  x += dpp<XOR1>(x); x += dpp<XOR2>(x); x += dpp<ROR4>(x); x += dpp<ROR8>(x); return xrow_sum(x);
}
__device__ __forceinline__ float rows_reduce4(f32x4 v) {
  auto a = __builtin_amdgcn_permlane32_swap(__float_as_uint(v[0]), __float_as_uint(v[2]), false, false);
  auto b = __builtin_amdgcn_permlane32_swap(__float_as_uint(v[1]), __float_as_uint(v[3]), false, false);
  const float u0 = __uint_as_float(a[0]) + __uint_as_float(a[1]), u1 = __uint_as_float(b[0]) + __uint_as_float(b[1]);
  auto c = __builtin_amdgcn_permlane16_swap(__float_as_uint(u0), __float_as_uint(u1), false, false);
  return __uint_as_float(c[0]) + __uint_as_float(c[1]);
}
__device__ __forceinline__ u32x4 ln8(u32x4 v) {
  float f[8];
#pragma unroll
  for (int i = 0; i < 4; ++i) { f[2 * i] = blo(v[i]); f[2 * i + 1] = bhi(v[i]); }
  const float mean = wave_sum(((f[0] + f[1]) + (f[2] + f[3])) + ((f[4] + f[5]) + (f[6] + f[7]))) * (1.f / 512.f);
  float ss = 0.f;
#pragma unroll
  for (int i = 0; i < 8; ++i) { const float d = f[i] - mean; ss = fmaf(d, d, ss); }
  const float var = wave_sum(ss) * (1.f / 512.f);
  const float mb = rbf(mean), inv = __builtin_amdgcn_rcpf(rbf(__builtin_amdgcn_sqrtf(rbf(rbf(var) + 1e-5f))));
  u32x4 o;
#pragma unroll
  for (int i = 0; i < 4; ++i) { const unsigned c = cvtpk(f[2 * i] - mb, f[2 * i + 1] - mb); o[i] = cvtpk(blo(c) * inv, bhi(c) * inv); }
  return o;
}
__device__ __forceinline__ u32x4 tr2(LAS unsigned char* p, int o0, int o1) {
  const u32x2 a = __builtin_bit_cast(u32x2, __builtin_amdgcn_ds_read_tr16_b64_v4i16((LAS s16x4*)(p + o0)));
  const u32x2 b = __builtin_bit_cast(u32x2, __builtin_amdgcn_ds_read_tr16_b64_v4i16((LAS s16x4*)(p + o1)));
  return u32x4{a[0], a[1], b[0], b[1]};
}
__device__ __forceinline__ f32x4 gemv64(LAS unsigned char* img, LAS unsigned char* bp, int g, int r) {
  LAS unsigned char* const a = img + (8 * g + (r >> 2)) * 128 + (r & 3) * 8;
  const u32x4 b0 = *(LAS u32x4*)(bp + g * 16), b1 = *(LAS u32x4*)(bp + 64 + g * 16);
  f32x4 z[4];
#pragma unroll
  for (int n = 0; n < 4; ++n) {
    z[n] = mfma16(tr2(a, n * 32, n * 32 + 512), b0, f32x4{0.f, 0.f, 0.f, 0.f});
    z[n] = mfma16(tr2(a, 4096 + n * 32, 4096 + n * 32 + 512), b1, z[n]);
  }
  return (r & 4) ? ((r & 2) ? z[3] : z[2]) : ((r & 2) ? z[1] : z[0]);
}
#define BAR() asm volatile("s_waitcnt lgkmcnt(0)\n\ts_barrier" ::: "memory")
#define FENCE() asm volatile("" ::: "memory")
#define VMW(N) do { __builtin_amdgcn_s_waitcnt(0x0F70 | ((N) & 15) | (((N) >> 4) << 14)); SCHED(); } while (0)

__device__ __forceinline__ unsigned rotr(unsigned x, unsigned n) { return __builtin_amdgcn_alignbit(x, x, n); }
__device__ __forceinline__ unsigned sum4(u32x4 w) { return w[0] + rotr(w[1], 8) + rotr(w[2], 16) + rotr(w[3], 24); }
__device__ __forceinline__ unsigned sum4_top(u32x4 w) {
  return (w[0] & 0xffffff00u) + __builtin_amdgcn_perm(0u, w[1], 0x0c030201u) + __builtin_amdgcn_perm(0u, w[2], 0x010c0302u) + __builtin_amdgcn_perm(0u, w[3], 0x02010c03u);
}
__device__ __forceinline__ u32x4 low_bytes(u32x4 w, unsigned d) {
  return u32x4{__builtin_amdgcn_perm(w[0], d, 0x07060500u), __builtin_amdgcn_perm(w[1], d, 0x07060503u), __builtin_amdgcn_perm(w[2], d, 0x07060502u), __builtin_amdgcn_perm(w[3], d, 0x07060501u)};
}
__device__ __forceinline__ u32x4 seal(f32x4 v, unsigned salt) { const u32x4 w = __builtin_bit_cast(u32x4, v); return low_bytes(w, salt - sum4_top(w)); }
__device__ __forceinline__ f32x4 vals(u32x4 w) { return __builtin_bit_cast(f32x4, w & 0xffffff00u); }
__device__ __forceinline__ f32x4 halves_sum(f32x4 v) {
  auto a = __builtin_amdgcn_permlane32_swap(__float_as_uint(v[0]), __float_as_uint(v[0]), false, false), b = __builtin_amdgcn_permlane32_swap(__float_as_uint(v[1]), __float_as_uint(v[1]), false, false);
  auto c = __builtin_amdgcn_permlane32_swap(__float_as_uint(v[2]), __float_as_uint(v[2]), false, false), d = __builtin_amdgcn_permlane32_swap(__float_as_uint(v[3]), __float_as_uint(v[3]), false, false);
  return f32x4{__uint_as_float(a[0]) + __uint_as_float(a[1]), __uint_as_float(b[0]) + __uint_as_float(b[1]), __uint_as_float(c[0]) + __uint_as_float(c[1]), __uint_as_float(d[0]) + __uint_as_float(d[1])};
}
#define SALT(g) ((unsigned)(g) * 0x9E3779B1u + 0x7F4A7C15u)
#define GIVE_UP(mask) do { --spin; mask = spin > 0 ? mask : 0u; asm volatile("" : "+v"(mask), "+v"(spin)); } while (0)
#define PIN(x) asm volatile("" : "+v"(x))

extern "C" __global__ void __launch_bounds__(512, 2)
fused_decode(const u16* a_x, const u16* a_k, const u16* a_v, const u16* a_wq, const u16* a_wo, const u16* a_w1, const u16* a_b1, const u16* a_w2, const u16* a_b2,
             u16* a_out, unsigned char* a_ws) {
  __shared__ __attribute__((aligned(16))) unsigned char smem[L_END];
  LAS unsigned char* const L = (LAS unsigned char*)smem;
#define ARGS() const CAS u64* kp_ = (const CAS u64*)__builtin_amdgcn_kernarg_segment_ptr(); asm volatile("" : "+s"(kp_)); \
  const u16* const x = (const u16*)kp_[0]; const u16* const Kc = (const u16*)kp_[1]; const u16* const Vc = (const u16*)kp_[2]; const u16* const wq = (const u16*)kp_[3]; \
  const u16* const wo = (const u16*)kp_[4]; const u16* const w1 = (const u16*)kp_[5]; const u16* const b1 = (const u16*)kp_[6]; const u16* const w2 = (const u16*)kp_[7]; \
  const u16* const b2 = (const u16*)kp_[8]; u16* const out = (u16*)kp_[9]; unsigned char* const ws = (unsigned char*)kp_[10]; \
  const auto rw = __builtin_amdgcn_make_buffer_rsrc(ws, 0, WS_END, RS3); \
  (void)x; (void)Kc; (void)Vc; (void)wq; (void)wo; (void)w1; (void)b1; (void)w2; (void)b2; (void)out; (void)rw
  const int lane = threadIdx.x & 63;
  int wave = __builtin_amdgcn_readfirstlane(threadIdx.x >> 6);
  const int xcd = blockIdx.x & 7, j = blockIdx.x >> 3, lw = xcd * 32 + j;
  const int g = lane >> 4, r = lane & 15;
  const unsigned box = WS_BOX + lw * BOX;
  const int pr = (xcd & 3) * 32 + j, ab = (pr >> 3) & 1, akv = pr >> 4, slot = (pr & 7) * 2 + (xcd >> 2);
  const bool mlp = j < 28;
  const int jm = mlp ? j : 0;
  float xres = 0.f, bias2 = 0.f;
  u32x4 zp = {0u, 0u, 0u, 0u};
  unsigned salt = 0;
  u64 gnext = 0;

  if (wave < 4) {
    ARGS();
    asm volatile("s_setprio 3" ::: "memory");
    SCHED();
    unsigned vq = (wave * 32 + (lane >> 3)) * 8192 + (lane & 7) * 16, v1 = (wave * 16 + (lane >> 3)) * 3584 + (lane & 7) * 16;
    unsigned vx = (wave & 1) * 1024 + lane * 16;
    unsigned qo = ((j & 3) * 128 * 4096 + xcd * 512 + (j >> 2) * 64) * 2, o1 = (xcd * 64 * 1792 + jm * 64) * 2, len1 = mlp ? 63u * 3584u + 128u : 0u;
    constexpr unsigned PSTR = 8192, WSTR = 2048;
    unsigned koff = wave * WSTR + (r >> 2) * 256 + g * 64 + (r & 3) * 16, voff = wave * WSTR + lane * 16, kvo = ((ab * 8 + akv) * SK + slot * 2048) * 256u;
    asm volatile("" : "+v"(vx), "+v"(vq), "+v"(v1), "+v"(koff), "+v"(voff), "+s"(qo), "+s"(o1), "+s"(len1), "+s"(kvo));
    {
      const u64 qb = (u64)wq + qo, b1_ = (u64)w1 + o1;
      const u32x4 rq = {(unsigned)qb, (unsigned)(qb >> 32) & 0xffffu, 127u * 8192u + 128u, (unsigned)RS3};
      const u32x4 r1 = {(unsigned)b1_, (unsigned)(b1_ >> 32) & 0xffffu, len1, (unsigned)RS3};
      const u32x4 rx = {(unsigned)(u64)x, (unsigned)((u64)x >> 32) & 0xffffu, 2048u, (unsigned)RS3};
      asm volatile("s_mov_b32 m0, %12\n\ts_nop 3\n\tbuffer_load_dwordx4 %10, %11, 0 offen lds\n\t"
                   "s_mov_b32 m0, %2\n\ts_nop 0\n\t"
                   "buffer_load_dwordx4 %0, %1, 0 offen lds\n\t"
                   "s_add_u32 m0, m0, 0x400\n\ts_nop 0\n\tbuffer_load_dwordx4 %0, %1, %3 offen lds\n\t"
                   "s_add_u32 m0, m0, 0x400\n\ts_nop 0\n\tbuffer_load_dwordx4 %0, %1, %4 offen lds\n\t"
                   "s_add_u32 m0, m0, 0x400\n\ts_nop 0\n\tbuffer_load_dwordx4 %0, %1, %5 offen lds\n\t"
                   "s_mov_b32 m0, %8\n\ts_nop 0\n\tbuffer_load_dwordx4 %6, %7, 0 offen lds\n\t"
                   "s_add_u32 m0, m0, 0x400\n\ts_nop 0\n\tbuffer_load_dwordx4 %6, %7, %9 offen lds"
                   :: "v"(vq), "s"(rq), "s"((unsigned)(size_t)(L + L_WQ) + wave * 4096), "s"(0x10000u), "s"(0x20000u), "s"(0x30000u),
                      "v"(v1), "s"(r1), "s"((unsigned)(size_t)(L + L_W1) + wave * 2048), "s"(8u * 3584u),
                      "v"(vx), "s"(rx), "s"((unsigned)(size_t)(L + L_XN) + wave * 1024) : "memory", "scc");
    }
    __builtin_amdgcn_s_setprio(0);
    const CAS u64* gp = (const CAS u64*)(ws + WS_G);
    asm volatile("" : "+s"(gp));
    const u64 gprev = *gp;
    const auto rk = __builtin_amdgcn_make_buffer_rsrc((char*)Kc + kvo, 0, 2048 * 256, RS3);
    const auto rv = __builtin_amdgcn_make_buffer_rsrc((char*)Vc + kvo, 0, 2048 * 256, RS3);
    u32x4 tb[4][8];
#define LOAD(B, I, RS, VO, T) do { tb[B][I] = __builtin_amdgcn_raw_buffer_load_b128(RS, (VO) + ((I) & 1) * 1024, (unsigned)(T) * 32768u + ((I) >> 1) * PSTR, AUX_NT); SCHED(); } while (0)
#define PRE(H) do { if ((H) == P0) asm volatile("s_waitcnt vmcnt(%0)\n\ts_barrier" :: "n"(6 + P0) : "memory");   \
    if ((H) > P0) VMW(PW - 1); \
    if ((H) == PW - 1) { FENCE(); *(volatile LAS int*)(L + L_F1 + wave * 4) = 1; SCHED(); }     \
    LOAD((H) >> 3, (H) & 7, rk, koff, (H) >> 3); } while (0)
#define PRE2(H) do { if ((H) < LK) PRE(H); } while (0)
#define PRE3(H) do { if ((H) >= LK) PRE(H); } while (0)
#define X8(M, H) do { M(H); M((H) + 1); M((H) + 2); M((H) + 3); M((H) + 4); M((H) + 5); M((H) + 6); M((H) + 7); } while (0)
    SCHED();
    X8(PRE, 0); X8(PRE, 8);
    { u64 g_ = gprev; asm volatile("" : "+s"(g_)); salt = SALT(g_ + 1); }
    X8(PRE2, 16); X8(PRE2, 24);
    const unsigned qsrc = WS_QG + akv * 16384 + (lane >> 5) * 4096 + (wave * 8 + ((lane >> 2) & 7)) * 128 + ab * 64 + (lane & 3) * 16;
    u32x4 qv[2];
    {
      const u32x4 rwq = {(unsigned)(u64)ws, (unsigned)((u64)ws >> 32) & 0xffffu, WS_END, (unsigned)RS3};
      asm volatile("s_waitcnt vmcnt(%5)\n\tbuffer_load_dwordx4 %0, %2, %3, 0 offen sc1\n\tbuffer_load_dwordx4 %1, %2, %3, %4 offen sc1"
                   : "=&v"(qv[0]), "=&v"(qv[1]) : "v"(qsrc), "s"(rwq), "s"(8192u), "n"(PW - 2) : "memory");
    }
    X8(PRE3, 16); X8(PRE3, 24);
    {
      const u64 b2_ = (u64)(w2 + jm * 64 * 512 + xcd * 64), bo = (u64)(wo + (xcd * 512 + (j >> 3) * 128) * 512 + (j & 7) * 64);
      const u32x4 r2 = {(unsigned)b2_, (unsigned)(b2_ >> 32) & 0xffffu, mlp ? 63u * 1024u + 128u : 0u, (unsigned)RS3};
      const u32x4 ro = {(unsigned)bo, (unsigned)(bo >> 32) & 0xffffu, 127u * 1024u + 128u, (unsigned)RS3};
#define DMA2(V, R, LB, S0, S1) asm volatile("s_waitcnt vmcnt(%5)\n\ts_mov_b32 m0, %2\n\ts_nop 0\n\tbuffer_load_dwordx4 %0, %1, %3 offen lds\n\t" \
                     "s_add_u32 m0, m0, 0x400\n\ts_waitcnt vmcnt(%5)\n\tbuffer_load_dwordx4 %0, %1, %4 offen lds" :: "v"(V), "s"(R), "s"(LB), "s"(S0), "s"(S1), "n"(PW - 1) : "memory", "scc")
      const unsigned v2 = (wave * 16 + (lane >> 3)) * 1024 + (lane & 7) * 16, vo = (lane >> 3) * 8192 + wave * 2048 + (lane & 7) * 16;
      DMA2(v2, r2, (unsigned)(size_t)(L + L_W2) + wave * 2048, 0u, 8192u);
      DMA2(vo, ro, (unsigned)(size_t)(L + L_WO) + wave * 4096, 0u, 65536u);
      DMA2(vo, ro, (unsigned)(size_t)(L + L_WO) + wave * 4096 + 2048, 1024u, 65536u + 1024u);
    }
    asm volatile("s_waitcnt vmcnt(%2)" : "+v"(qv[0]), "+v"(qv[1]) : "n"(32 - LK + 6));
    {
#define QBAD() (__builtin_amdgcn_ballot_w64(((sum4(qv[0]) ^ salt) | (sum4(qv[1]) ^ salt)) != 0) != 0)
      if (__builtin_expect(QBAD(), 0)) {
#pragma unroll 1
        for (unsigned spin = 0; spin < SPIN_CAP; ++spin) {
          FENCE();
          qv[0] = __builtin_amdgcn_raw_buffer_load_b128(rw, qsrc, 0, AUX_SC1); qv[1] = __builtin_amdgcn_raw_buffer_load_b128(rw, qsrc + 8192, 0, AUX_SC1);
          if (!QBAD()) break;
        }
      }
      const f32x4 q4 = halves_sum(vals(qv[0]) + vals(qv[1]));
      *(LAS u32x2*)(L + L_Q + wave * 256 + (lane & 31) * 8) = u32x2{cvtpk(q4[0], q4[1]), cvtpk(q4[2], q4[3])};
    }
    BAR();
    const int head = (r & 3) ^ ((r & 4) ? 3 : 0);
    const u32x4 qd = *(LAS u32x4*)(L + L_Q + head * 256 + g * 64 + (r >> 2) * 16);
    LAS unsigned char* sr = L + L_S + wave * 8192 + g * 128 + head * 32;
    LAS unsigned char* sw = sr + (r & 4) * 4 + (r >> 3) * 4;
    float m = -3.0e38f, y[4];
#define QP(B, P) do { const f32x4 z4 = {0.f, 0.f, 0.f, 0.f}, da = mfma16(tb[B][2 * (P)], qd, z4), db = mfma16(tb[B][2 * (P) + 1], qd, z4); \
    const float a0 = (r & 4) ? da[1] : da[0], a1 = (r & 4) ? da[3] : da[2], b0 = (r & 4) ? db[1] : db[0], b1 = (r & 4) ? db[3] : db[2]; \
    y[P] = ((r & 8) ? b1 : a0) + dpp<ROR8>((r & 8) ? a1 : b0); } while (0)
#define QE(T) do { const float z0 = ((r & 4) ? y[2] : y[0]) + dpp<HMIR>((r & 4) ? y[0] : y[2]), z1 = ((r & 4) ? y[3] : y[1]) + dpp<HMIR>((r & 4) ? y[1] : y[3]); \
    m = fmaxf(m, fmaxf(z0, z1)); *(LAS float*)(sw + (T) * 512) = z0; *(LAS float*)(sw + (T) * 512 + 8) = z1; } while (0)
#define QS(B, T) do { QP(B, 0); QP(B, 1); QP(B, 2); QP(B, 3); QE(T); SCHED(); } while (0)
#define CATCH(I) do { if ((I) < AH) LOAD(0, I, rk, koff, 4); } while (0)
    QS(0, 0); CATCH(0); CATCH(1); CATCH(2); CATCH(3); CATCH(4); CATCH(5); CATCH(6); CATCH(7);
    QS(1, 1); QS(2, 2); QS(3, 3);
    VMW(AH);
    *(volatile LAS int*)(L + L_PRG + wave * 4) = 1;
    LAS unsigned char* la = L + L_A + (wave * 4 + r) * A_STR + g * 16;
    PIN(la);
    int lag = 0;
#define WAITK() do { asm volatile("s_cmp_gt_i32 %0, 0\n\ts_cbranch_scc1 .Lw%=\n\ts_waitcnt vmcnt(%1)\n.Lw%=:\n\ts_waitcnt vmcnt(%2)" :: "s"(lag), "n"(WK - 1), "n"(WK) : "scc"); SCHED(); } while (0)
#define CTL0() const u64 now64_ = __builtin_amdgcn_s_memrealtime(); const u32x2 lv_ = *(volatile LAS u32x2*)(L + L_LINE); SCHED()
#define CTL1(N) do { asm volatile("" :: "s"(now64_)); const unsigned now_ = (unsigned)now64_; lag = __builtin_amdgcn_readfirstlane((now_ - lv_[1] - (N) * CB) & lv_[0]); \
    if (wave == 0) *(volatile LAS u32x2*)(L + L_CK) = u32x2{(unsigned)(N), now_}; SCHED(); } while (0)
#define STEP(H, RS, VO, RN, VN, TN) do { WAITK(); \
    if ((H) + AH < 16) LOAD(((H) + AH) >> 3, ((H) + AH) & 7, RS, VO, t + (((H) + AH) >> 3)); else LOAD(0, ((H) + AH) & 7, RN, VN, TN); } while (0)
#define STEP2(H, ...) do { STEP(H, __VA_ARGS__); STEP((H) + 1, __VA_ARGS__); } while (0)
#define KT(B, ...) do { CTL0(); STEP2(8 * (B), __VA_ARGS__); CTL1(t + (B)); QP(B, 0); SCHED(); STEP2(8 * (B) + 2, __VA_ARGS__); QP(B, 1); SCHED(); \
    STEP2(8 * (B) + 4, __VA_ARGS__); QP(B, 2); SCHED(); STEP2(8 * (B) + 6, __VA_ARGS__); QP(B, 3); QE(t + (B)); SCHED(); } while (0)
#pragma unroll 1
    for (int t = 4; t < 16; t += 2) {
      const bool last = t == 14;
      const auto rn = __builtin_amdgcn_make_buffer_rsrc((last ? (char*)Vc : (char*)Kc) + kvo, 0, 2048 * 256, RS3);
      const unsigned vn = last ? voff : koff, tn = (t + 2) & 15;
      KT(0, rk, koff, rn, vn, tn); KT(1, rk, koff, rn, vn, tn);
    }
    m = fmaxf(m, dpp<HMIR>(m)); m = fmaxf(m, dpp<ROR8>(m));
    const float mc = xrow_max(m) * C2;
    float l = 0.f;
    f32x4 acc[8];
#pragma unroll
    for (int e = 0; e < 8; ++e) acc[e] = f32x4{0.f, 0.f, 0.f, 0.f};
    u32x4 pf, vt[8];
#define SP(T) do { const f32x4 s0 = *(LAS f32x4*)(sr + (T) * 512), s1 = *(LAS f32x4*)(sr + (T) * 512 + 16); float p[8]; \
    _Pragma("unroll") for (int i = 0; i < 4; ++i) { p[i] = __builtin_amdgcn_exp2f(fmaf(s0[i], C2, -mc)); p[4 + i] = __builtin_amdgcn_exp2f(fmaf(s1[i], C2, -mc)); } \
    l += ((p[0] + p[1]) + (p[2] + p[3])) + ((p[4] + p[5]) + (p[6] + p[7])); \
    pf = u32x4{cvtpk(p[0], p[1]), cvtpk(p[2], p[3]), cvtpk(p[4], p[5]), cvtpk(p[6], p[7])}; SCHED(); } while (0)
#define VP(B, P) do { _Pragma("unroll") for (int w = 0; w < 4; ++w) { vt[2 * w][P] = __builtin_amdgcn_perm(tb[B][2 * (P) + 1][w], tb[B][2 * (P)][w], 0x05040100u); \
      vt[2 * w + 1][P] = __builtin_amdgcn_perm(tb[B][2 * (P) + 1][w], tb[B][2 * (P)][w], 0x07060302u); } } while (0)
#define VE() do { _Pragma("unroll") for (int e = 0; e < 8; ++e) acc[e] = mfma16(vt[e], pf, acc[e]); SCHED(); } while (0)
#define VT(B, ...) do { CTL0(); STEP2(8 * (B), __VA_ARGS__); CTL1(16 + t + (B)); VP(B, 0); SP(t + (B)); STEP2(8 * (B) + 2, __VA_ARGS__); VP(B, 1); SCHED(); \
    STEP2(8 * (B) + 4, __VA_ARGS__); VP(B, 2); SCHED(); STEP2(8 * (B) + 6, __VA_ARGS__); VP(B, 3); VE(); } while (0)
#pragma unroll 1
    for (int t = 0; t < TE; t += 2) { VT(0, rv, voff, rv, voff, t + 2); VT(1, rv, voff, rv, voff, t + 2); }
#define EL(N) do { if ((N) < 8 * (16 - TE)) { if ((N) >= WE) VMW(WE - 1); LOAD((N) >> 3, (N) & 7, rv, voff, TE + ((N) >> 3)); } } while (0)
#define EP(N) do { if ((N) >= AH && (N) < WE) EL(N); } while (0)
    X8(EP, 0); X8(EP, 8); X8(EP, 16); X8(EP, 24);
#define ET(B) do { if (TE + (B) < 16) { SP(TE + (B)); EL(WE + 8 * (B)); EL(WE + 8 * (B) + 1); VP(B, 0); SCHED(); EL(WE + 8 * (B) + 2); EL(WE + 8 * (B) + 3); VP(B, 1); SCHED(); \
      EL(WE + 8 * (B) + 4); EL(WE + 8 * (B) + 5); VP(B, 2); SCHED(); EL(WE + 8 * (B) + 6); EL(WE + 8 * (B) + 7); VP(B, 3); VE(); } } while (0)
    ET(0); ET(1); ET(2); ET(3);
    l = xrow_sum(l);
    if (r < 4) {
#pragma unroll
      for (int e = 0; e < 8; ++e) *(LAS f32x4*)(la + e * 64) = acc[e];
      *(LAS float*)(L + L_M + r * 16 + wave * 4) = mc; *(LAS float*)(L + L_L + r * 16 + wave * 4) = l;
    }
    BAR();
  }
  asm volatile("" : "+s"(wave));
  if (wave >= 4) {
    __builtin_amdgcn_s_setprio(2);
    ARGS();
    const int n = wave - 4;
    unsigned seen = 0;
    if (n == 2) seen = __builtin_amdgcn_raw_buffer_load_b32(rw, lane < 24 ? box + lane * 128 : lane < 28 ? WS_QG + xcd * 16384 + (j & 3) * 4096 + ((j >> 2) * 4 + (lane & 3)) * 128
                                                              : (lane < 32 && mlp) ? WS_HG + (j * 8 + xcd) * 512 + (lane & 3) * 128 : (lane == 32 && blockIdx.x < 16) ? WS_TS + blockIdx.x * 128 : OOB, 0, 0);
    gnext = *(const CAS u64*)(ws + WS_G) + 1;
    *(volatile LAS int*)(L + L_PRG + n * 4) = 0; *(volatile LAS int*)(L + L_F1 + n * 4) = 0; *(volatile LAS int*)(L + L_F1 + 16 + n * 4) = 0; *(volatile LAS int*)(L + L_CK + n * 4) = 0;
    unsigned qdst = r < 2 ? WS_QG + xcd * 16384 + (j & 3) * 4096 + ((j >> 2) * 4 + n) * 128 + r * 64 + g * 16 : OOB;
    unsigned hdst = (r < 2 && mlp) ? WS_HG + (j * 8 + xcd) * 512 + n * 128 + r * 64 + g * 16 : OOB;
    PIN(qdst); PIN(hdst);
    BAR();
    u32x4 xr = *(LAS u32x4*)(L + L_XN + n * 1024 + lane * 16);
#pragma unroll 1
    for (int it = n >> 1; it >= 0; --it) xr = ln8(xr);
    *(LAS u32x4*)(L + L_XN + n * 1024 + lane * 16) = xr;
    salt = SALT(gnext);
    asm volatile("s_waitcnt lgkmcnt(0)" : "+s"(salt), "+s"(gnext) :: "memory");
    *(volatile LAS int*)(L + L_F1 + 16 + n * 4) = 1;
#pragma unroll 1
    for (unsigned spin = 0; spin < 16 * SPIN_CAP; ++spin) {
      const u32x4 p = *(volatile LAS u32x4*)(L + L_F1) & *(volatile LAS u32x4*)(L + L_F1 + 16);
      if ((p[0] & p[1] & p[2] & p[3]) != 0) break;
    }
    FENCE();
    const int ao = (8 * g + (r >> 2)) * 128 + (r & 3) * 8 + n * 32;
    LAS unsigned char* const bp = L + L_XN + (r & 1) * 1024 + g * 16;
    f32x4 qv = {0.f, 0.f, 0.f, 0.f}, hv = {0.f, 0.f, 0.f, 0.f};
#pragma unroll
    for (int k = 0; k < 4; ++k) qv = mfma16(tr2(L + L_WQ + ao, k * 4096, k * 4096 + 512), *(LAS u32x4*)(bp + (j & 3) * 256 + k * 64), qv);
#pragma unroll
    for (int k = 0; k < 2; ++k) hv = mfma16(tr2(L + L_W1 + ao, k * 4096, k * 4096 + 512), *(LAS u32x4*)(bp + 2048 + xcd * 128 + k * 64), hv);
    __builtin_amdgcn_raw_buffer_store_b128(seal(qv, salt), rw, qdst, 0, AUX_SC1);
    __builtin_amdgcn_raw_buffer_store_b128(seal(hv, salt), rw, hdst, 0, AUX_SC1);
    if (n == 3) {
      u64 pc; unsigned back, len;
      asm volatile("s_getpc_b64 %0\n.Lpc%=:\n\ts_mov_b32 %1, .Lpc%=-fused_decode\n\ts_mov_b32 %2, .Lfd_last-fused_decode" : "=s"(pc), "=s"(back), "=s"(len));
      seen = __builtin_amdgcn_raw_buffer_load_b128(__builtin_amdgcn_make_buffer_rsrc((void*)(pc - back), 0, len, RS3), lane < 40 ? j * 640 + lane * 16 : OOB, 0, 0)[0];
    }
    asm volatile("s_barrier" :: "v"(seen) : "memory");
  }
  asm volatile("" : "+s"(wave));
  if (wave == 6) {
    ARGS();
    __builtin_amdgcn_s_setprio(0);
#pragma unroll 1
    for (unsigned spin = 0; spin < SPIN_CAP; ++spin) {
      const u32x4 p = *(volatile LAS u32x4*)(L + L_PRG);
      if (min(min(p[0], p[1]), min(p[2], p[3])) != 0) break;
      __builtin_amdgcn_s_sleep(16);
    }
    SCHED();
    __builtin_amdgcn_s_setprio(3);
    const int hcol = ((lane >> 3) & 3) * 16 + (lane & 3) * 4;
    const u32x2 bb = *(const GAS u32x2*)(b1 + jm * 64 + hcol);
    const int ocol = xcd * 64 + (j & 15) * 4 + g;
    xres = blo(((const GAS u16*)x)[(lane & 1) * 512 + ocol]);
    bias2 = blo(((const GAS u16*)b2)[ocol]);
    u32x4 v[4];
#pragma unroll 1
    for (unsigned spin = 0; spin < SPIN_CAP; ++spin) {
      FENCE();
#pragma unroll
      for (int k = 0; k < 4; ++k) v[k] = __builtin_amdgcn_raw_buffer_load_b128(rw, WS_HG + jm * 4096 + k * 1024 + lane * 16, 0, AUX_SC1);
      if (__builtin_amdgcn_ballot_w64(((sum4(v[0]) ^ salt) | (sum4(v[1]) ^ salt) | (sum4(v[2]) ^ salt) | (sum4(v[3]) ^ salt)) != 0) == 0) break;
      __builtin_amdgcn_s_sleep(16);
    }
    SCHED();
    __builtin_amdgcn_s_setprio(0);
    const f32x4 hs = halves_sum((vals(v[0]) + vals(v[1])) + (vals(v[2]) + vals(v[3])));
    float hh[4];
#pragma unroll
    for (int i = 0; i < 4; ++i) {
      const float z = rbf(rbf(hs[i]) + ((i & 1) ? bhi(bb[i >> 1]) : blo(bb[i >> 1])));
      const float u = 0.7978845608028654f * (z + 0.044715f * z * z * z);
      hh[i] = z * __builtin_amdgcn_rcpf(1.f + __builtin_amdgcn_exp2f(-2.f * LOG2E * u));
    }
    *(LAS u32x2*)(L + L_H + ((lane >> 2) & 1) * 128 + hcol * 2) = u32x2{cvtpk(hh[0], hh[1]), cvtpk(hh[2], hh[3])};
    zp = seal(gemv64(L + L_W2, L + L_H + (r & 1) * 128, g, r), salt);
    PIN(zp); PIN(xres); PIN(bias2);
  }
  asm volatile("" : "+s"(wave));
  ARGS();
  if (wave == 7) {
    const bool sample = (j & 7) == ((xcd + 3 * (j >> 3)) & 7);
#pragma unroll 1
    for (int n = 4, k = 0; n + CL < 16 + TE; n += CS, ++k) {
      u32x2 ck;
#pragma unroll 1
      for (unsigned spin = 0; spin < SPIN_CAP; ++spin) { ck = *(volatile LAS u32x2*)(L + L_CK); if ((int)ck[0] >= n) break; __builtin_amdgcn_s_sleep(4); }
      const unsigned tk = ck[1] - (ck[0] - n) * CB;
      __builtin_amdgcn_raw_buffer_store_b128(seal(__builtin_bit_cast(f32x4, u32x4{tk << 8, 0u, 0u, 0u}), salt), rw, (sample && lane == 0) ? WS_TS + k * 512 + ((j >> 3) * 8 + xcd) * 16 : OOB, 0, AUX_SC1);
#pragma unroll 1
      for (unsigned spin = 0; spin < SPIN_CAP; ++spin) { if (*(volatile LAS int*)(L + L_CK) >= n + CL) break; __builtin_amdgcn_s_sleep(4); }
      const u32x4 v = __builtin_amdgcn_raw_buffer_load_b128(rw, lane < 32 ? WS_TS + k * 512 + lane * 16 : OOB, 0, AUX_SC1);
      const bool ok = lane < 32 && sum4(v) == salt;
      const int cnt = __builtin_popcountll(__builtin_amdgcn_ballot_w64(ok));
      const float mean = wave_sum(ok ? (float)((int)((v[0] & 0xffffff00u) - (tk << 8)) >> 8) : 0.f) * __builtin_amdgcn_rcpf((float)cnt);
      if (cnt >= 16) *(volatile LAS u32x2*)(L + L_LINE) = u32x2{~0u, tk + (int)mean - n * CB};
    }
  }
  if (wave >= 4) {
    unsigned dst1 = wave == 7 ? OOB : wave == 6 ? ((r < 8 && mlp) ? box + BX_ZB + (r >> 1) * 128 + (r & 1) * 64 + g * 16 : OOB)
                                                : WS_BOX + (akv * 32 + slot * 2 + ab) * BOX + BX_PA + ((wave & 1) * 64 + lane) * 16;
    const int hh = (wave & 1) * 2 + (lane >> 5);
    LAS unsigned char *lm = L + L_M + hh * 16, *la = L + L_A + hh * A_STR + (lane & 31) * 16;
    PIN(dst1); PIN(lm); PIN(la);
    asm volatile("" :: "s"(ws));
    BAR();
    if (wave < 6) {
      const f32x4 mm = *(LAS f32x4*)lm, ll = *(LAS f32x4*)(lm + (L_L - L_M));
      f32x4 a[4];
#pragma unroll
      for (int w = 0; w < 4; ++w) a[w] = *(LAS f32x4*)(la + w * 4 * A_STR);
      SCHED();
      const float M = max3(max3(mm[0], mm[1], mm[2]), mm[3], mm[3]);
      float wt[4];
#pragma unroll
      for (int w = 0; w < 4; ++w) wt[w] = __builtin_amdgcn_exp2f(mm[w] - M);
      const float Ls = fmaf(ll[3], wt[3], fmaf(ll[2], wt[2], fmaf(ll[1], wt[1], ll[0] * wt[0])));
      f32x4 o = a[0] * wt[0];
#pragma unroll
      for (int w = 1; w < 4; ++w) o = __builtin_elementwise_fma(a[w], f32x4{wt[w], wt[w], wt[w], wt[w]}, o);
      o *= __builtin_amdgcn_rcpf(Ls);
      const unsigned lse = __float_as_uint(M + __builtin_amdgcn_logf(Ls));
      const u32x4 w = __builtin_bit_cast(u32x4, o);
      const unsigned sw = sum4_top(w), so = (unsigned)__builtin_amdgcn_mov_dpp((int)sw, XOR1, 0xf, 0xf, true);
      zp = low_bytes(w, (lane & 1) ? lse : salt - sw - rotr(so + lse, 5));
    }
    __builtin_amdgcn_raw_buffer_store_b128(zp, rw, dst1, 0, AUX_SC1);
  }
  unsigned psrc = WS_BOX + (xcd * 32 + (lane >> 3) * 2 + (wave >> 2)) * BOX + BX_PA + (j >> 3) * 512 + (wave & 3) * 128 + (lane & 7) * 16;
  unsigned dst2 = r < 2 ? box + BX_SB + (wave & 3) * 128 + r * 64 + g * 16 : OOB;
  LAS unsigned char* lo = L + L_O + (wave >> 2) * 256 + ((wave & 3) * 32 + (lane & 7) * 4 + g) * 2;
  LAS unsigned char* lb = L + L_O + (r & 1) * 256 + g * 16;
  PIN(dst2); PIN(lo); PIN(lb);
  LAS unsigned char* const aw = L + L_WO + (8 * g + (r >> 2)) * 128 + (r & 3) * 8 + (wave & 3) * 32;
  u32x4 wa[4];
#pragma unroll
  for (int k = 0; k < 4; ++k) wa[k] = tr2(aw, k * 4096, k * 4096 + 512);
  {
    u32x4 v0, v1;
    int spin = SPIN_CAP;
    unsigned even = (lane & 1) - 1u;
    PIN(even);
    __builtin_amdgcn_s_sleep(PD);
#pragma unroll 1
    do {
      FENCE();
      v0 = __builtin_amdgcn_raw_buffer_load_b128(rw, psrc, 0, AUX_SC1);
      v1 = __builtin_amdgcn_raw_buffer_load_b128(rw, psrc + 16 * BOX, 0, AUX_SC1);
      const unsigned s0 = sum4(v0), s1 = sum4(v1);
      const unsigned c0 = s0 + rotr((unsigned)__builtin_amdgcn_mov_dpp((int)s0, XOR1, 0xf, 0xf, true), 5), c1 = s1 + rotr((unsigned)__builtin_amdgcn_mov_dpp((int)s1, XOR1, 0xf, 0xf, true), 5);
      if (__builtin_amdgcn_ballot_w64((((c0 ^ salt) | (c1 ^ salt)) & even) != 0) == 0) break;
      __builtin_amdgcn_s_sleep(PSL);
      GIVE_UP(even);
    } while (true);
#define LOWB(v) __uint_as_float(__builtin_amdgcn_perm(__builtin_amdgcn_perm(v[1], v[2], 0x04000000u), __builtin_amdgcn_perm(v[3], v[0], 0x00000400u), 0x07060100u))
    const float e0 = dpp<ODD>(LOWB(v0)), e1 = dpp<ODD>(LOWB(v1));
    const float M = xrow_max(max3(max3(e0, e1, dpp<ROR8>(e0)), dpp<ROR8>(e1), e0));
    const float w0 = __builtin_amdgcn_exp2f(e0 - M), w1_ = __builtin_amdgcn_exp2f(e1 - M);
    float Ws = w0 + w1_; Ws += dpp<ROR8>(Ws); Ws = xrow_sum(Ws);
    float o = rows_reduce4(__builtin_elementwise_fma(vals(v1), f32x4{w1_, w1_, w1_, w1_}, vals(v0) * w0));
    o += dpp<ROR8>(o);
    o *= __builtin_amdgcn_rcpf(Ws);
    *(LAS u16*)lo = (u16)cvtpk(o, o);
  }
  BAR();
  if (wave < 4) {
    u32x4 ob[4];
#pragma unroll
    for (int k = 0; k < 4; ++k) ob[k] = *(LAS u32x4*)(lb + k * 64);
    SCHED();
    const f32x4 z4 = {0.f, 0.f, 0.f, 0.f};
    const f32x4 s = mfma16(wa[1], ob[1], mfma16(wa[0], ob[0], z4)) + mfma16(wa[3], ob[3], mfma16(wa[2], ob[2], z4));
    __builtin_amdgcn_raw_buffer_store_b128(seal(s, salt), rw, dst2, 0, AUX_SC1);
  }
  if (wave == 6 && j < 16) {
    const unsigned in = (j >> 2) * 128 + (lane & 1) * 64 + (j & 3) * 16;
    const unsigned zsrc = lane < 56 ? WS_BOX + (xcd * 32 + (lane >> 1)) * BOX + BX_ZB + in : OOB;
    const unsigned ssrc = WS_BOX + ((lane >> 3) * 32 + ((lane >> 1) & 3) * 8 + xcd) * BOX + BX_SB + in;
    u32x4 v;
    int spin = SPIN_CAP;
    unsigned need = lane < 56 ? ~0u : 0u;
#pragma unroll 1
    do {
      FENCE();
      v = __builtin_amdgcn_raw_buffer_load_b128(rw, zsrc, 0, AUX_SC1);
      if (__builtin_amdgcn_ballot_w64(((sum4(v) ^ salt) & need) != 0) == 0) break;
      GIVE_UP(need);
    } while (true);
    float z = rows_reduce4(vals(v));
    z += dpp<XOR2>(z); z += dpp<ROR4>(z); z += dpp<ROR8>(z);
    z = rbf(rbf(z) + bias2);
    float gate = rbf(z * __builtin_amdgcn_rcpf(1.f + __builtin_amdgcn_exp2f(-LOG2E * z)));
    unsigned odst = r < 2 ? (r * 512 + xcd * 64 + j * 4 + g) * 2 : OOB;
    PIN(gate); PIN(odst);
    spin = SPIN_CAP; need = ~0u;
#pragma unroll 1
    do {
      FENCE();
      v = __builtin_amdgcn_raw_buffer_load_b128(rw, ssrc, 0, AUX_SC1);
      if (__builtin_amdgcn_ballot_w64(((sum4(v) ^ salt) & need) != 0) == 0) break;
      GIVE_UP(need);
    } while (true);
    float s = rows_reduce4(vals(v));
    s += dpp<XOR2>(s); s += dpp<ROR4>(s); s += dpp<ROR8>(s);
    const float res = xres + rbf(rbf(s) * gate);
    const auto rout = __builtin_amdgcn_make_buffer_rsrc(out, 0, 2048, RS3);
    __builtin_amdgcn_raw_buffer_store_b16((u16)cvtpk(res, res), rout, odst, 0, 0);
    __builtin_amdgcn_raw_buffer_store_b64(u32x2{(unsigned)gnext, (unsigned)(gnext >> 32)}, rw, (blockIdx.x == 0 && lane == 0) ? WS_G : OOB, 0, AUX_SC1);
    asm volatile(".Lfd_last:");
  }
}

extern "C" void kernel_launch(void* const* d_in, const int* in_sizes, int n_in, void* d_out, int out_size, void* d_ws, size_t ws_size, hipStream_t stream) {
  fused_decode<<<256, 512, 0, stream>>>((const u16*)d_in[0], (const u16*)d_in[1], (const u16*)d_in[2], (const u16*)d_in[3], (const u16*)d_in[4],
                                        (const u16*)d_in[6], (const u16*)d_in[7], (const u16*)d_in[8], (const u16*)d_in[9], (u16*)d_out, (unsigned char*)d_ws);
}
```

```cpp
#include <hip/hip_runtime.h>
#include <stdint.h>

typedef unsigned short u16;
typedef unsigned long long u64;
typedef unsigned u32x2 __attribute__((ext_vector_type(2)));
typedef unsigned u32x4 __attribute__((ext_vector_type(4)));
typedef float f32x2 __attribute__((ext_vector_type(2)));
typedef float f32x4 __attribute__((ext_vector_type(4)));
typedef short s16x4 __attribute__((ext_vector_type(4)));
typedef short bf16x8 __attribute__((ext_vector_type(8)));
typedef __bf16 bf16x2_t __attribute__((ext_vector_type(2)));
#define GAS __attribute__((address_space(1)))
#define LAS __attribute__((address_space(3)))
#define CAS __attribute__((address_space(4)))

#define WK 5
#define WE 8
#define TE 14
#define PW 10
#define P0 1
#define LK 28
#define CB 112
#define CS 12
#define CL 2
#define PD 16
#define PSL 2
constexpr int AH = WK + 1;
static_assert((16 + TE - CL - 5) / CS < 4 && AH <= 8 && WE >= 8 && (TE == 12 || TE == 14) && P0 < PW - 1 && PW <= 16 && LK >= 16 && LK <= 32, "");

constexpr int SK = 32768;
constexpr int AUX_SC1 = 16, AUX_NT = 2, RS3 = 0x00020000;
constexpr unsigned WS_G = 0, WS_QG = 4096, WS_HG = WS_QG + 8 * 16384, WS_TS = WS_HG + 224 * 512, WS_BOX = WS_TS + 4 * 512;
constexpr unsigned BOX = 3072 + 128, BX_PA = 0, BX_SB = 2048, BX_ZB = 2560, WS_END = WS_BOX + 256 * BOX;
constexpr int L_WQ = 0, L_WO = 16384, L_W1 = 32768, L_W2 = 40960, L_XN = 49152, L_Y = L_XN + 2048, L_Q = 53248, L_H = 54272, L_O = 54528,
              L_M = 55040, L_L = 55104, L_PRG = 55168, L_F1 = 55184, L_CK = 55216, L_LINE = 55224, L_A = 55296, A_STR = 528, L_S = L_A + 16 * A_STR, L_DUMP = L_S + 32768, L_END = L_DUMP + 1024;
constexpr unsigned OOB = 0x80000000u;
constexpr unsigned SPIN_CAP = 1u << 19;
constexpr float LOG2E = 1.4426950408889634f, C2 = 0.08838834764831845f * LOG2E;

#define SCHED() __builtin_amdgcn_sched_barrier(0)

__device__ __forceinline__ f32x4 mfma16(u32x4 a, u32x4 b, f32x4 c) {
  return __builtin_amdgcn_mfma_f32_16x16x32_bf16(__builtin_bit_cast(bf16x8, a), __builtin_bit_cast(bf16x8, b), c, 0, 0, 0);
}
__device__ __forceinline__ unsigned cvtpk(float lo, float hi) { return __builtin_bit_cast(unsigned, __builtin_convertvector(f32x2{lo, hi}, bf16x2_t)); }
__device__ __forceinline__ float blo(unsigned u) { return __uint_as_float(u << 16); }
__device__ __forceinline__ float bhi(unsigned u) { return __uint_as_float(u & 0xffff0000u); }
__device__ __forceinline__ float rbf(float x) { return blo(cvtpk(x, x)); }
template <int CTRL> __device__ __forceinline__ float dpp(float x) {
  return __builtin_bit_cast(float, __builtin_amdgcn_mov_dpp(__builtin_bit_cast(int, x), CTRL, 0xf, 0xf, true));
}
constexpr int XOR1 = 0xB1, XOR2 = 0x4E, ROR4 = 0x124, ROR8 = 0x128, HMIR = 0x141, ODD = 0xF5;
__device__ __forceinline__ float max3(float a, float b, float c) { float d; asm("v_max3_f32 %0, %1, %2, %3" : "=v"(d) : "v"(a), "v"(b), "v"(c)); return d; }
__device__ __forceinline__ float xrow_max(float x) {
  auto s = __builtin_amdgcn_permlane16_swap(__float_as_uint(x), __float_as_uint(x), false, false);
  x = max3(__uint_as_float(s[0]), __uint_as_float(s[1]), __uint_as_float(s[1]));
  auto t = __builtin_amdgcn_permlane32_swap(__float_as_uint(x), __float_as_uint(x), false, false);
  return max3(__uint_as_float(t[0]), __uint_as_float(t[1]), __uint_as_float(t[1]));
}
__device__ __forceinline__ float xrow_sum(float x) {
  auto s = __builtin_amdgcn_permlane16_swap(__float_as_uint(x), __float_as_uint(x), false, false);
  x = __uint_as_float(s[0]) + __uint_as_float(s[1]);
  auto t = __builtin_amdgcn_permlane32_swap(__float_as_uint(x), __float_as_uint(x), false, false);
  return __uint_as_float(t[0]) + __uint_as_float(t[1]);
}
__device__ __forceinline__ float wave_sum(float x) {
  x += dpp<XOR1>(x); x += dpp<XOR2>(x); x += dpp<ROR4>(x); x += dpp<ROR8>(x); return xrow_sum(x);
}
__device__ __forceinline__ float rows_reduce4(f32x4 v) {
  auto a = __builtin_amdgcn_permlane32_swap(__float_as_uint(v[0]), __float_as_uint(v[2]), false, false);
  auto b = __builtin_amdgcn_permlane32_swap(__float_as_uint(v[1]), __float_as_uint(v[3]), false, false);
  const float u0 = __uint_as_float(a[0]) + __uint_as_float(a[1]), u1 = __uint_as_float(b[0]) + __uint_as_float(b[1]);
  auto c = __builtin_amdgcn_permlane16_swap(__float_as_uint(u0), __float_as_uint(u1), false, false);
  return __uint_as_float(c[0]) + __uint_as_float(c[1]);
}
__device__ __forceinline__ u32x4 ln8(u32x4 v) {
  float f[8];
#pragma unroll
  for (int i = 0; i < 4; ++i) { f[2 * i] = blo(v[i]); f[2 * i + 1] = bhi(v[i]); }
  const float mean = wave_sum(((f[0] + f[1]) + (f[2] + f[3])) + ((f[4] + f[5]) + (f[6] + f[7]))) * (1.f / 512.f);
  float ss = 0.f;
#pragma unroll
  for (int i = 0; i < 8; ++i) { const float d = f[i] - mean; ss = fmaf(d, d, ss); }
  const float var = wave_sum(ss) * (1.f / 512.f);
  const float mb = rbf(mean), inv = __builtin_amdgcn_rcpf(rbf(__builtin_amdgcn_sqrtf(rbf(rbf(var) + 1e-5f))));
  u32x4 o;
#pragma unroll
  for (int i = 0; i < 4; ++i) { const unsigned c = cvtpk(f[2 * i] - mb, f[2 * i + 1] - mb); o[i] = cvtpk(blo(c) * inv, bhi(c) * inv); }
  return o;
}
__device__ __forceinline__ u32x4 tr2(LAS unsigned char* p, int o0, int o1) {
  const u32x2 a = __builtin_bit_cast(u32x2, __builtin_amdgcn_ds_read_tr16_b64_v4i16((LAS s16x4*)(p + o0)));
  const u32x2 b = __builtin_bit_cast(u32x2, __builtin_amdgcn_ds_read_tr16_b64_v4i16((LAS s16x4*)(p + o1)));
  return u32x4{a[0], a[1], b[0], b[1]};
}
__device__ __forceinline__ f32x4 gemv64(LAS unsigned char* img, LAS unsigned char* bp, int g, int r) {
  LAS unsigned char* const a = img + (8 * g + (r >> 2)) * 128 + (r & 3) * 8;
  const u32x4 b0 = *(LAS u32x4*)(bp + g * 16), b1 = *(LAS u32x4*)(bp + 64 + g * 16);
  f32x4 z[4];
#pragma unroll
  for (int n = 0; n < 4; ++n) {
    z[n] = mfma16(tr2(a, n * 32, n * 32 + 512), b0, f32x4{0.f, 0.f, 0.f, 0.f});
    z[n] = mfma16(tr2(a, 4096 + n * 32, 4096 + n * 32 + 512), b1, z[n]);
  }
  return (r & 4) ? ((r & 2) ? z[3] : z[2]) : ((r & 2) ? z[1] : z[0]);
}
#define BAR() asm volatile("s_waitcnt lgkmcnt(0)\n\ts_barrier" ::: "memory")
#define FENCE() asm volatile("" ::: "memory")
#define VMW(N) do { __builtin_amdgcn_s_waitcnt(0x0F70 | ((N) & 15) | (((N) >> 4) << 14)); SCHED(); } while (0)

__device__ __forceinline__ unsigned rotr(unsigned x, unsigned n) { return __builtin_amdgcn_alignbit(x, x, n); }
__device__ __forceinline__ unsigned sum4(u32x4 w) { return w[0] + rotr(w[1], 8) + rotr(w[2], 16) + rotr(w[3], 24); }
struct Sel { unsigned t1 = 0x0c030201u, t2 = 0x010c0302u, t3 = 0x02010c03u, b0 = 0x07060500u, b1 = 0x07060503u, b2 = 0x07060502u, b3 = 0x07060501u; };
__device__ __forceinline__ unsigned sum4_top(u32x4 w, const Sel& k = Sel()) {
  return (w[0] & 0xffffff00u) + __builtin_amdgcn_perm(w[1], w[1], k.t1) + __builtin_amdgcn_perm(w[2], w[2], k.t2) + __builtin_amdgcn_perm(w[3], w[3], k.t3);
}
__device__ __forceinline__ u32x4 low_bytes(u32x4 w, unsigned d, const Sel& k = Sel()) {
  return u32x4{__builtin_amdgcn_perm(w[0], d, k.b0), __builtin_amdgcn_perm(w[1], d, k.b1), __builtin_amdgcn_perm(w[2], d, k.b2), __builtin_amdgcn_perm(w[3], d, k.b3)};
}
__device__ __forceinline__ u32x4 seal(f32x4 v, unsigned salt, const Sel& k = Sel()) { const u32x4 w = __builtin_bit_cast(u32x4, v); return low_bytes(w, salt - sum4_top(w, k), k); }
#define PIN_SEL(k) asm volatile("" : "+s"(k.t1), "+s"(k.t2), "+s"(k.t3), "+s"(k.b0), "+s"(k.b1), "+s"(k.b2), "+s"(k.b3))
__device__ __forceinline__ f32x4 vals(u32x4 w) { return __builtin_bit_cast(f32x4, w & 0xffffff00u); }
__device__ __forceinline__ f32x4 halves_sum(f32x4 v) {
  auto a = __builtin_amdgcn_permlane32_swap(__float_as_uint(v[0]), __float_as_uint(v[0]), false, false), b = __builtin_amdgcn_permlane32_swap(__float_as_uint(v[1]), __float_as_uint(v[1]), false, false);
  auto c = __builtin_amdgcn_permlane32_swap(__float_as_uint(v[2]), __float_as_uint(v[2]), false, false), d = __builtin_amdgcn_permlane32_swap(__float_as_uint(v[3]), __float_as_uint(v[3]), false, false);
  return f32x4{__uint_as_float(a[0]) + __uint_as_float(a[1]), __uint_as_float(b[0]) + __uint_as_float(b[1]), __uint_as_float(c[0]) + __uint_as_float(c[1]), __uint_as_float(d[0]) + __uint_as_float(d[1])};
}
#define SALT(g) ((unsigned)(g) * 0x9E3779B1u + 0x7F4A7C15u)
#define GIVE_UP(mask) do { --spin; mask = spin > 0 ? mask : 0u; asm volatile("" : "+v"(mask), "+v"(spin)); } while (0)
#define PIN(x) asm volatile("" : "+v"(x))

extern "C" __global__ void __launch_bounds__(512, 2)
fused_decode(const u16* a_x, const u16* a_k, const u16* a_v, const u16* a_wq, const u16* a_wo, const u16* a_w1, const u16* a_b1, const u16* a_w2, const u16* a_b2,
             u16* a_out, unsigned char* a_ws) {
  __shared__ __attribute__((aligned(16))) unsigned char smem[L_END];
  LAS unsigned char* const L = (LAS unsigned char*)smem;
  asm volatile("s_load_dword s96, %0, 0x0\n\ts_load_dword s97, %0, 0x50" :: "s"(__builtin_amdgcn_kernarg_segment_ptr()) : "s96", "s97");
#define ARGS() const CAS u64* kp_ = (const CAS u64*)__builtin_amdgcn_kernarg_segment_ptr(); asm volatile("" : "+s"(kp_)); \
  const u16* const x = (const u16*)kp_[0]; const u16* const Kc = (const u16*)kp_[1]; const u16* const Vc = (const u16*)kp_[2]; const u16* const wq = (const u16*)kp_[3]; \
  const u16* const wo = (const u16*)kp_[4]; const u16* const w1 = (const u16*)kp_[5]; const u16* const b1 = (const u16*)kp_[6]; const u16* const w2 = (const u16*)kp_[7]; \
  const u16* const b2 = (const u16*)kp_[8]; u16* const out = (u16*)kp_[9]; unsigned char* const ws = (unsigned char*)kp_[10]; \
  const auto rw = __builtin_amdgcn_make_buffer_rsrc(ws, 0, WS_END, RS3); \
  (void)x; (void)Kc; (void)Vc; (void)wq; (void)wo; (void)w1; (void)b1; (void)w2; (void)b2; (void)out; (void)rw
  const int lane = threadIdx.x & 63;
  int wave = __builtin_amdgcn_readfirstlane(threadIdx.x >> 6);
  const int xcd = blockIdx.x & 7, j = blockIdx.x >> 3, lw = xcd * 32 + j;
  {
    asm volatile("s_cmp_lg_u32 %0, 7\n\ts_cbranch_scc1 .Lnp%=\n\ts_getpc_b64 s[92:93]\n.Lpc%=:\n\ts_sub_u32 s92, s92, .Lpc%=-fused_decode\n\ts_subb_u32 s93, s93, 0\n\t"
                 "s_mov_b32 s94, .Lfd_last-fused_decode\n\ts_mov_b32 s95, 0x20000\n\ts_mov_b32 m0, %2\n\ts_nop 0\n\tbuffer_load_dwordx4 %1, s[92:95], 0 offen lds\n.Lnp%=:"
                 :: "s"(wave), "v"(lane < 40 ? j * 640 + lane * 16 : OOB), "s"((unsigned)(size_t)(L + L_DUMP)) : "memory", "scc", "s92", "s93", "s94", "s95");
  }
  const int g = lane >> 4, r = lane & 15;
  const unsigned box = WS_BOX + lw * BOX;
  const int pr = (xcd & 3) * 32 + j, ab = (pr >> 3) & 1, akv = pr >> 4, slot = (pr & 7) * 2 + (xcd >> 2);
  const bool mlp = j < 28;
  const int jm = mlp ? j : 0;
  float xres = 0.f, bias2 = 0.f;
  u32x4 zp = {0u, 0u, 0u, 0u};
  unsigned salt = 0;
  u64 gnext = 0;

  if (wave < 4) {
    ARGS();
    asm volatile("s_setprio 3" ::: "memory");
    SCHED();
    unsigned vq = (wave * 32 + (lane >> 3)) * 8192 + (lane & 7) * 16, v1 = (wave * 16 + (lane >> 3)) * 3584 + (lane & 7) * 16;
    unsigned vx = (wave & 1) * 1024 + lane * 16;
    unsigned qo = ((j & 3) * 128 * 4096 + xcd * 512 + (j >> 2) * 64) * 2, o1 = (xcd * 64 * 1792 + jm * 64) * 2, len1 = mlp ? 63u * 3584u + 128u : 0u;
    constexpr unsigned PSTR = 8192, WSTR = 2048;
    unsigned koff = wave * WSTR + (r >> 2) * 256 + g * 64 + (r & 3) * 16, voff = wave * WSTR + lane * 16, kvo = ((ab * 8 + akv) * SK + slot * 2048) * 256u;
    asm volatile("" : "+v"(vx), "+v"(vq), "+v"(v1), "+v"(koff), "+v"(voff), "+s"(qo), "+s"(o1), "+s"(len1), "+s"(kvo));
    {
      const u64 qb = (u64)wq + qo, b1_ = (u64)w1 + o1;
      const u32x4 rq = {(unsigned)qb, (unsigned)(qb >> 32) & 0xffffu, 127u * 8192u + 128u, (unsigned)RS3};
      const u32x4 r1 = {(unsigned)b1_, (unsigned)(b1_ >> 32) & 0xffffu, len1, (unsigned)RS3};
      const u32x4 rx = {(unsigned)(u64)x, (unsigned)((u64)x >> 32) & 0xffffu, 2048u, (unsigned)RS3};
      asm volatile("s_mov_b32 m0, %12\n\ts_nop 3\n\tbuffer_load_dwordx4 %10, %11, 0 offen lds\n\t"
                   "s_mov_b32 m0, %2\n\ts_nop 0\n\t"
                   "buffer_load_dwordx4 %0, %1, 0 offen nt lds\n\t"
                   "s_add_u32 m0, m0, 0x400\n\ts_nop 0\n\tbuffer_load_dwordx4 %0, %1, %3 offen nt lds\n\t"
                   "s_add_u32 m0, m0, 0x400\n\ts_nop 0\n\tbuffer_load_dwordx4 %0, %1, %4 offen nt lds\n\t"
                   "s_add_u32 m0, m0, 0x400\n\ts_nop 0\n\tbuffer_load_dwordx4 %0, %1, %5 offen nt lds\n\t"
                   "s_mov_b32 m0, %8\n\ts_nop 0\n\tbuffer_load_dwordx4 %6, %7, 0 offen nt lds\n\t"
                   "s_add_u32 m0, m0, 0x400\n\ts_nop 0\n\tbuffer_load_dwordx4 %6, %7, %9 offen nt lds"
                   :: "v"(vq), "s"(rq), "s"((unsigned)(size_t)(L + L_WQ) + wave * 4096), "s"(0x10000u), "s"(0x20000u), "s"(0x30000u),
                      "v"(v1), "s"(r1), "s"((unsigned)(size_t)(L + L_W1) + wave * 2048), "s"(8u * 3584u),
                      "v"(vx), "s"(rx), "s"((unsigned)(size_t)(L + L_XN) + wave * 1024) : "memory", "scc");
    }
    __builtin_amdgcn_s_setprio(0);
    const CAS u64* gp = (const CAS u64*)(ws + WS_G);
    asm volatile("" : "+s"(gp));
    const u64 gprev = *gp;
    const auto rk = __builtin_amdgcn_make_buffer_rsrc((char*)Kc + kvo, 0, 2048 * 256, RS3);
    const auto rv = __builtin_amdgcn_make_buffer_rsrc((char*)Vc + kvo, 0, 2048 * 256, RS3);
    u32x4 tb[4][8];
#define LOAD(B, I, RS, VO, T) do { tb[B][I] = __builtin_amdgcn_raw_buffer_load_b128(RS, (VO) + ((I) & 1) * 1024, (unsigned)(T) * 32768u + ((I) >> 1) * PSTR, AUX_NT); SCHED(); } while (0)
#define PRE(H) do { if ((H) == P0) asm volatile("s_waitcnt vmcnt(%0)\n\ts_barrier" :: "n"(6 + P0) : "memory");   \
    if ((H) > P0) VMW(PW - 1); \
    if ((H) == PW - 1) { FENCE(); *(volatile LAS int*)(L + L_F1 + wave * 4) = 1; SCHED(); }     \
    LOAD((H) >> 3, (H) & 7, rk, koff, (H) >> 3); } while (0)
#define PRE2(H) do { if ((H) < LK) PRE(H); } while (0)
#define PRE3(H) do { if ((H) >= LK) PRE(H); } while (0)
#define X8(M, H) do { M(H); M((H) + 1); M((H) + 2); M((H) + 3); M((H) + 4); M((H) + 5); M((H) + 6); M((H) + 7); } while (0)
    SCHED();
    X8(PRE, 0); X8(PRE, 8);
    { u64 g_ = gprev; asm volatile("" : "+s"(g_)); salt = SALT(g_ + 1); }
    X8(PRE2, 16); X8(PRE2, 24);
    const unsigned qsrc = WS_QG + akv * 16384 + (lane >> 5) * 4096 + (wave * 8 + ((lane >> 2) & 7)) * 128 + ab * 64 + (lane & 3) * 16;
    u32x4 qv[2];
    {
      const u32x4 rwq = {(unsigned)(u64)ws, (unsigned)((u64)ws >> 32) & 0xffffu, WS_END, (unsigned)RS3};
      asm volatile("s_waitcnt vmcnt(%5)\n\tbuffer_load_dwordx4 %0, %2, %3, 0 offen sc1\n\tbuffer_load_dwordx4 %1, %2, %3, %4 offen sc1"
                   : "=&v"(qv[0]), "=&v"(qv[1]) : "v"(qsrc), "s"(rwq), "s"(8192u), "n"(PW - 2) : "memory");
    }
    X8(PRE3, 16); X8(PRE3, 24);
    {
      const u64 b2_ = (u64)(w2 + jm * 64 * 512 + xcd * 64), bo = (u64)(wo + (xcd * 512 + (j >> 3) * 128) * 512 + (j & 7) * 64);
      const u32x4 r2 = {(unsigned)b2_, (unsigned)(b2_ >> 32) & 0xffffu, mlp ? 63u * 1024u + 128u : 0u, (unsigned)RS3};
      const u32x4 ro = {(unsigned)bo, (unsigned)(bo >> 32) & 0xffffu, 127u * 1024u + 128u, (unsigned)RS3};
#define DMA2(V, R, LB, S0, S1) asm volatile("s_waitcnt vmcnt(%5)\n\ts_mov_b32 m0, %2\n\ts_nop 0\n\tbuffer_load_dwordx4 %0, %1, %3 offen nt lds\n\t" \
                     "s_add_u32 m0, m0, 0x400\n\ts_waitcnt vmcnt(%5)\n\tbuffer_load_dwordx4 %0, %1, %4 offen nt lds" :: "v"(V), "s"(R), "s"(LB), "s"(S0), "s"(S1), "n"(PW - 1) : "memory", "scc")
      const unsigned v2 = (wave * 16 + (lane >> 3)) * 1024 + (lane & 7) * 16, vo = (lane >> 3) * 8192 + wave * 2048 + (lane & 7) * 16;
      DMA2(v2, r2, (unsigned)(size_t)(L + L_W2) + wave * 2048, 0u, 8192u);
      DMA2(vo, ro, (unsigned)(size_t)(L + L_WO) + wave * 4096, 0u, 65536u);
      DMA2(vo, ro, (unsigned)(size_t)(L + L_WO) + wave * 4096 + 2048, 1024u, 65536u + 1024u);
    }
    asm volatile("s_waitcnt vmcnt(%2)" : "+v"(qv[0]), "+v"(qv[1]) : "n"(32 - LK + 6));
    {
#define QBAD() (__builtin_amdgcn_ballot_w64(((sum4(qv[0]) ^ salt) | (sum4(qv[1]) ^ salt)) != 0) != 0)
      if (__builtin_expect(QBAD(), 0)) {
#pragma unroll 1
        for (unsigned spin = 0; spin < SPIN_CAP; ++spin) {
          FENCE();
          qv[0] = __builtin_amdgcn_raw_buffer_load_b128(rw, qsrc, 0, AUX_SC1); qv[1] = __builtin_amdgcn_raw_buffer_load_b128(rw, qsrc + 8192, 0, AUX_SC1);
          if (!QBAD()) break;
        }
      }
      const f32x4 q4 = halves_sum(vals(qv[0]) + vals(qv[1]));
      *(LAS u32x2*)(L + L_Q + wave * 256 + (lane & 31) * 8) = u32x2{cvtpk(q4[0], q4[1]), cvtpk(q4[2], q4[3])};
    }
    BAR();
    const int head = (r & 3) ^ ((r & 4) ? 3 : 0);
    const u32x4 qd = *(LAS u32x4*)(L + L_Q + head * 256 + g * 64 + (r >> 2) * 16);
    LAS unsigned char* sr = L + L_S + wave * 8192 + g * 128 + head * 32;
    LAS unsigned char* sw = sr + (r & 4) * 4 + (r >> 3) * 4;
    float m = -3.0e38f, y[4];
    f32x4 da, db;
#define QM(B, P) do { const f32x4 z4 = {0.f, 0.f, 0.f, 0.f}; da = mfma16(tb[B][2 * (P)], qd, z4); db = mfma16(tb[B][2 * (P) + 1], qd, z4); } while (0)
#define QX(P) do { const float a0 = (r & 4) ? da[1] : da[0], a1 = (r & 4) ? da[3] : da[2], b0 = (r & 4) ? db[1] : db[0], b1 = (r & 4) ? db[3] : db[2]; \
    y[P] = ((r & 8) ? b1 : a0) + dpp<ROR8>((r & 8) ? a1 : b0); } while (0)
#define QP(B, P) do { QM(B, P); QX(P); } while (0)
#define QE(T) do { const float z0 = ((r & 4) ? y[2] : y[0]) + dpp<HMIR>((r & 4) ? y[0] : y[2]), z1 = ((r & 4) ? y[3] : y[1]) + dpp<HMIR>((r & 4) ? y[1] : y[3]); \
    m = fmaxf(m, fmaxf(z0, z1)); *(LAS float*)(sw + (T) * 512) = z0; *(LAS float*)(sw + (T) * 512 + 8) = z1; } while (0)
#define QS(B, T) do { QP(B, 0); QP(B, 1); QP(B, 2); QP(B, 3); QE(T); SCHED(); } while (0)
#define CATCH(I) do { if ((I) < AH) LOAD(0, I, rk, koff, 4); } while (0)
    QS(0, 0); CATCH(0); CATCH(1); CATCH(2); CATCH(3); CATCH(4); CATCH(5); CATCH(6); CATCH(7);
    QS(1, 1); QS(2, 2); QS(3, 3);
    VMW(AH);
    *(volatile LAS int*)(L + L_PRG + wave * 4) = 1;
    LAS unsigned char* la = L + L_A + (wave * 4 + r) * A_STR + g * 16;
    PIN(la);
    int lag = 0;
#define WAITK() do { asm volatile("s_cmp_gt_i32 %0, 0\n\ts_cbranch_scc1 .Lw%=\n\ts_waitcnt vmcnt(%1)\n.Lw%=:\n\ts_waitcnt vmcnt(%2)" :: "s"(lag), "n"(WK - 1), "n"(WK) : "scc"); SCHED(); } while (0)
#define CTL0() const u64 now64_ = __builtin_amdgcn_s_memrealtime(); const u32x2 lv_ = *(volatile LAS u32x2*)(L + L_LINE); SCHED()
#define CTL1(N) do { asm volatile("" :: "s"(now64_)); const unsigned now_ = (unsigned)now64_; lag = __builtin_amdgcn_readfirstlane((now_ - lv_[1] - (N) * CB) & lv_[0]); \
    if (wave == 0) *(volatile LAS u32x2*)(L + L_CK) = u32x2{(unsigned)(N), now_}; SCHED(); } while (0)
#define STEP(H, RS, VO, RN, VN, TN) do { WAITK(); \
    if ((H) + AH < 16) LOAD(((H) + AH) >> 3, ((H) + AH) & 7, RS, VO, t + (((H) + AH) >> 3)); else LOAD(0, ((H) + AH) & 7, RN, VN, TN); } while (0)
#define STEP2(H, ...) do { STEP(H, __VA_ARGS__); STEP((H) + 1, __VA_ARGS__); } while (0)
#define KT(B, ...) do { CTL0(); STEP2(8 * (B), __VA_ARGS__); QM(B, 0); SCHED(); CTL1(t + (B)); STEP2(8 * (B) + 2, __VA_ARGS__); QX(0); SCHED(); QM(B, 1); SCHED(); \
    STEP2(8 * (B) + 4, __VA_ARGS__); QX(1); SCHED(); QM(B, 2); SCHED(); STEP2(8 * (B) + 6, __VA_ARGS__); QX(2); SCHED(); QM(B, 3); SCHED(); QX(3); QE(t + (B)); SCHED(); } while (0)
#pragma unroll 1
    for (int t = 4; t < 16; t += 2) {
      const bool last = t == 14;
      const auto rn = __builtin_amdgcn_make_buffer_rsrc((last ? (char*)Vc : (char*)Kc) + kvo, 0, 2048 * 256, RS3);
      const unsigned vn = last ? voff : koff, tn = (t + 2) & 15;
      KT(0, rk, koff, rn, vn, tn); KT(1, rk, koff, rn, vn, tn);
    }
    m = fmaxf(m, dpp<HMIR>(m)); m = fmaxf(m, dpp<ROR8>(m));
    const float mc = xrow_max(m) * C2;
    float l = 0.f;
    f32x4 acc[8];
#pragma unroll
    for (int e = 0; e < 8; ++e) acc[e] = f32x4{0.f, 0.f, 0.f, 0.f};
    u32x4 pf, vt[8];
#define SP(T) do { const f32x4 s0 = *(LAS f32x4*)(sr + (T) * 512), s1 = *(LAS f32x4*)(sr + (T) * 512 + 16); float p[8]; \
    _Pragma("unroll") for (int i = 0; i < 4; ++i) { p[i] = __builtin_amdgcn_exp2f(fmaf(s0[i], C2, -mc)); p[4 + i] = __builtin_amdgcn_exp2f(fmaf(s1[i], C2, -mc)); } \
    l += ((p[0] + p[1]) + (p[2] + p[3])) + ((p[4] + p[5]) + (p[6] + p[7])); \
    pf = u32x4{cvtpk(p[0], p[1]), cvtpk(p[2], p[3]), cvtpk(p[4], p[5]), cvtpk(p[6], p[7])}; SCHED(); } while (0)
#define VP(B, P) do { _Pragma("unroll") for (int w = 0; w < 4; ++w) { vt[2 * w][P] = __builtin_amdgcn_perm(tb[B][2 * (P) + 1][w], tb[B][2 * (P)][w], 0x05040100u); \
      vt[2 * w + 1][P] = __builtin_amdgcn_perm(tb[B][2 * (P) + 1][w], tb[B][2 * (P)][w], 0x07060302u); } } while (0)
#define VE() do { _Pragma("unroll") for (int e = 0; e < 8; ++e) acc[e] = mfma16(vt[e], pf, acc[e]); SCHED(); } while (0)
#define VT(B, ...) do { CTL0(); STEP2(8 * (B), __VA_ARGS__); CTL1(16 + t + (B)); VP(B, 0); SP(t + (B)); STEP2(8 * (B) + 2, __VA_ARGS__); VP(B, 1); SCHED(); \
    STEP2(8 * (B) + 4, __VA_ARGS__); VP(B, 2); SCHED(); STEP2(8 * (B) + 6, __VA_ARGS__); VP(B, 3); VE(); } while (0)
#pragma unroll 1
    for (int t = 0; t < TE; t += 2) { VT(0, rv, voff, rv, voff, t + 2); VT(1, rv, voff, rv, voff, t + 2); }
#define EL(N) do { if ((N) < 8 * (16 - TE)) { if ((N) >= WE) VMW(WE - 1); LOAD((N) >> 3, (N) & 7, rv, voff, TE + ((N) >> 3)); } } while (0)
#define EP(N) do { if ((N) >= AH && (N) < WE) EL(N); } while (0)
    X8(EP, 0); X8(EP, 8); X8(EP, 16); X8(EP, 24);
#define ET(B) do { if (TE + (B) < 16) { SP(TE + (B)); EL(WE + 8 * (B)); EL(WE + 8 * (B) + 1); VP(B, 0); SCHED(); EL(WE + 8 * (B) + 2); EL(WE + 8 * (B) + 3); VP(B, 1); SCHED(); \
      EL(WE + 8 * (B) + 4); EL(WE + 8 * (B) + 5); VP(B, 2); SCHED(); EL(WE + 8 * (B) + 6); EL(WE + 8 * (B) + 7); VP(B, 3); VE(); } } while (0)
    ET(0); ET(1); ET(2); ET(3);
    l = xrow_sum(l);
    if (r < 4) {
#pragma unroll
      for (int e = 0; e < 8; ++e) *(LAS f32x4*)(la + e * 64) = acc[e];
      *(LAS float*)(L + L_M + r * 16 + wave * 4) = mc; *(LAS float*)(L + L_L + r * 16 + wave * 4) = l;
    }
    BAR();
  }
  asm volatile("" : "+s"(wave));
  if (wave >= 4) {
    __builtin_amdgcn_s_setprio(2);
    ARGS();
    const int n = wave - 4;
    unsigned seen = 0;
    if (n == 2) seen = __builtin_amdgcn_raw_buffer_load_b32(rw, lane < 24 ? box + lane * 128 : lane < 28 ? WS_QG + xcd * 16384 + (j & 3) * 4096 + ((j >> 2) * 4 + (lane & 3)) * 128
                                                              : (lane < 32 && mlp) ? WS_HG + (j * 8 + xcd) * 512 + (lane & 3) * 128 : (lane == 32 && blockIdx.x < 16) ? WS_TS + blockIdx.x * 128 : OOB, 0, 0);
    gnext = *(const CAS u64*)(ws + WS_G) + 1;
    *(volatile LAS int*)(L + L_PRG + n * 4) = 0; *(volatile LAS int*)(L + L_F1 + n * 4) = 0; *(volatile LAS int*)(L + L_F1 + 16 + n * 4) = 0; *(volatile LAS int*)(L + L_CK + n * 4) = 0;
    unsigned qdst = r < 2 ? WS_QG + xcd * 16384 + (j & 3) * 4096 + ((j >> 2) * 4 + n) * 128 + r * 64 + g * 16 : OOB;
    unsigned hdst = (r < 2 && mlp) ? WS_HG + (j * 8 + xcd) * 512 + n * 128 + r * 64 + g * 16 : OOB;
    PIN(qdst); PIN(hdst);
    BAR();
    u32x4 xr = *(LAS u32x4*)(L + L_XN + n * 1024 + lane * 16);
#pragma unroll 1
    for (int it = n >> 1; it >= 0; --it) xr = ln8(xr);
    *(LAS u32x4*)(L + L_XN + n * 1024 + lane * 16) = xr;
    salt = SALT(gnext);
    asm volatile("s_waitcnt lgkmcnt(0)" : "+s"(salt), "+s"(gnext) :: "memory");
    *(volatile LAS int*)(L + L_F1 + 16 + n * 4) = 1;
#pragma unroll 1
    for (unsigned spin = 0; spin < 16 * SPIN_CAP; ++spin) {
      const u32x4 p = *(volatile LAS u32x4*)(L + L_F1) & *(volatile LAS u32x4*)(L + L_F1 + 16);
      if ((p[0] & p[1] & p[2] & p[3]) != 0) break;
    }
    FENCE();
    const int ao = (8 * g + (r >> 2)) * 128 + (r & 3) * 8 + n * 32;
    LAS unsigned char* const bp = L + L_XN + (r & 1) * 1024 + g * 16;
    f32x4 qv = {0.f, 0.f, 0.f, 0.f}, hv = {0.f, 0.f, 0.f, 0.f};
#pragma unroll
    for (int k = 0; k < 4; ++k) qv = mfma16(tr2(L + L_WQ + ao, k * 4096, k * 4096 + 512), *(LAS u32x4*)(bp + (j & 3) * 256 + k * 64), qv);
#pragma unroll
    for (int k = 0; k < 2; ++k) hv = mfma16(tr2(L + L_W1 + ao, k * 4096, k * 4096 + 512), *(LAS u32x4*)(bp + 2048 + xcd * 128 + k * 64), hv);
    __builtin_amdgcn_raw_buffer_store_b128(seal(qv, salt), rw, qdst, 0, AUX_SC1);
    __builtin_amdgcn_raw_buffer_store_b128(seal(hv, salt), rw, hdst, 0, AUX_SC1);
    asm volatile("s_barrier" :: "v"(seen) : "memory");
  }
  asm volatile("" : "+s"(wave));
  if (wave == 6) {
    ARGS();
    __builtin_amdgcn_s_setprio(0);
#pragma unroll 1
    for (unsigned spin = 0; spin < SPIN_CAP; ++spin) {
      const u32x4 p = *(volatile LAS u32x4*)(L + L_PRG);
      if (min(min(p[0], p[1]), min(p[2], p[3])) != 0) break;
      __builtin_amdgcn_s_sleep(16);
    }
    SCHED();
    __builtin_amdgcn_s_setprio(3);
    const int hcol = ((lane >> 3) & 3) * 16 + (lane & 3) * 4;
    const u32x2 bb = *(const GAS u32x2*)(b1 + jm * 64 + hcol);
    const int ocol = xcd * 64 + (j & 15) * 4 + g;
    xres = blo(((const GAS u16*)x)[(lane & 1) * 512 + ocol]);
    bias2 = blo(((const GAS u16*)b2)[ocol]);
    u32x4 v[4];
#pragma unroll 1
    for (unsigned spin = 0; spin < SPIN_CAP; ++spin) {
      FENCE();
#pragma unroll
      for (int k = 0; k < 4; ++k) v[k] = __builtin_amdgcn_raw_buffer_load_b128(rw, WS_HG + jm * 4096 + k * 1024 + lane * 16, 0, AUX_SC1);
      if (__builtin_amdgcn_ballot_w64(((sum4(v[0]) ^ salt) | (sum4(v[1]) ^ salt) | (sum4(v[2]) ^ salt) | (sum4(v[3]) ^ salt)) != 0) == 0) break;
      __builtin_amdgcn_s_sleep(16);
    }
    SCHED();
    __builtin_amdgcn_s_setprio(0);
    const f32x4 hs = halves_sum((vals(v[0]) + vals(v[1])) + (vals(v[2]) + vals(v[3])));
    float hh[4];
#pragma unroll
    for (int i = 0; i < 4; ++i) {
      const float z = rbf(rbf(hs[i]) + ((i & 1) ? bhi(bb[i >> 1]) : blo(bb[i >> 1])));
      const float u = 0.7978845608028654f * (z + 0.044715f * z * z * z);
      hh[i] = z * __builtin_amdgcn_rcpf(1.f + __builtin_amdgcn_exp2f(-2.f * LOG2E * u));
    }
    *(LAS u32x2*)(L + L_H + ((lane >> 2) & 1) * 128 + hcol * 2) = u32x2{cvtpk(hh[0], hh[1]), cvtpk(hh[2], hh[3])};
    zp = seal(gemv64(L + L_W2, L + L_H + (r & 1) * 128, g, r), salt);
    PIN(zp); PIN(xres); PIN(bias2);
  }
  asm volatile("" : "+s"(wave));
  ARGS();
  if (wave == 7) {
    const bool sample = (j & 7) == ((xcd + 3 * (j >> 3)) & 7);
#pragma unroll 1
    for (int n = 4, k = 0; n + CL < 16 + TE; n += CS, ++k) {
      u32x2 ck;
#pragma unroll 1
      for (unsigned spin = 0; spin < SPIN_CAP; ++spin) { ck = *(volatile LAS u32x2*)(L + L_CK); if ((int)ck[0] >= n) break; __builtin_amdgcn_s_sleep(4); }
      const unsigned tk = ck[1] - (ck[0] - n) * CB;
      __builtin_amdgcn_raw_buffer_store_b128(seal(__builtin_bit_cast(f32x4, u32x4{tk << 8, 0u, 0u, 0u}), salt), rw, (sample && lane == 0) ? WS_TS + k * 512 + ((j >> 3) * 8 + xcd) * 16 : OOB, 0, AUX_SC1);
#pragma unroll 1
      for (unsigned spin = 0; spin < SPIN_CAP; ++spin) { if (*(volatile LAS int*)(L + L_CK) >= n + CL) break; __builtin_amdgcn_s_sleep(4); }
      const u32x4 v = __builtin_amdgcn_raw_buffer_load_b128(rw, lane < 32 ? WS_TS + k * 512 + lane * 16 : OOB, 0, AUX_SC1);
      const bool ok = lane < 32 && sum4(v) == salt;
      const int cnt = __builtin_popcountll(__builtin_amdgcn_ballot_w64(ok));
      const float mean = wave_sum(ok ? (float)((int)((v[0] & 0xffffff00u) - (tk << 8)) >> 8) : 0.f) * __builtin_amdgcn_rcpf((float)cnt);
      if (cnt >= 16) *(volatile LAS u32x2*)(L + L_LINE) = u32x2{~0u, tk + (int)mean - n * CB};
    }
  }
  if (wave >= 4) {
    unsigned dst1 = wave == 7 ? OOB : wave == 6 ? ((r < 8 && mlp) ? box + BX_ZB + (r >> 1) * 128 + (r & 1) * 64 + g * 16 : OOB)
                                                : WS_BOX + (akv * 32 + slot * 2 + ab) * BOX + BX_PA + ((wave & 1) * 64 + lane) * 16;
    const int hh = (wave & 1) * 2 + (lane >> 5);
    LAS unsigned char *lm = L + L_M + hh * 16, *la = L + L_A + hh * A_STR + (lane & 31) * 16;
    Sel k;
    const bool odd = __builtin_amdgcn_inverse_ballot_w64(0xAAAAAAAAAAAAAAAAull);
    PIN(dst1); PIN(lm); PIN(la); PIN_SEL(k);
    asm volatile("" :: "s"(ws));
    BAR();
    if (wave < 6) {
      const f32x4 mm = *(LAS f32x4*)lm, ll = *(LAS f32x4*)(lm + (L_L - L_M));
      f32x4 a[4];
#pragma unroll
      for (int w = 0; w < 4; ++w) a[w] = *(LAS f32x4*)(la + w * 4 * A_STR);
      SCHED();
      const float M = max3(max3(mm[0], mm[1], mm[2]), mm[3], mm[3]);
      float wt[4];
#pragma unroll
      for (int w = 0; w < 4; ++w) wt[w] = __builtin_amdgcn_exp2f(mm[w] - M);
      const float Ls = fmaf(ll[3], wt[3], fmaf(ll[2], wt[2], fmaf(ll[1], wt[1], ll[0] * wt[0])));
      f32x4 o = a[0] * wt[0];
#pragma unroll
      for (int w = 1; w < 4; ++w) o = __builtin_elementwise_fma(a[w], f32x4{wt[w], wt[w], wt[w], wt[w]}, o);
      o *= __builtin_amdgcn_rcpf(Ls);
      const unsigned lse = __float_as_uint(M + __builtin_amdgcn_logf(Ls));
      const u32x4 w = __builtin_bit_cast(u32x4, o);
      const unsigned sw = sum4_top(w, k), so = (unsigned)__builtin_amdgcn_mov_dpp((int)sw, XOR1, 0xf, 0xf, true);
      zp = low_bytes(w, odd ? lse : salt - sw - rotr(so + lse, 5), k);
    }
    __builtin_amdgcn_raw_buffer_store_b128(zp, rw, dst1, 0, AUX_SC1);
  }
  unsigned psrc = WS_BOX + (xcd * 32 + (lane >> 3) * 2 + (wave >> 2)) * BOX + BX_PA + (j >> 3) * 512 + (wave & 3) * 128 + (lane & 7) * 16;
  unsigned dst2 = r < 2 ? box + BX_SB + (wave & 3) * 128 + r * 64 + g * 16 : OOB;
  LAS unsigned char* lo = L + L_O + (wave >> 2) * 256 + ((wave & 3) * 32 + (lane & 7) * 4 + g) * 2;
  LAS unsigned char* lb = L + L_O + (r & 1) * 256 + g * 16;
  Sel k;
  unsigned l0 = 0x04000000u, l1 = 0x00000400u, l2 = 0x07060100u;
  PIN(dst2); PIN(lo); PIN(lb); PIN_SEL(k);
  asm volatile("" : "+s"(l0), "+s"(l1), "+s"(l2));
  LAS unsigned char* const aw = L + L_WO + (8 * g + (r >> 2)) * 128 + (r & 3) * 8 + (wave & 3) * 32;
  u32x4 wa[4];
#pragma unroll
  for (int k = 0; k < 4; ++k) wa[k] = tr2(aw, k * 4096, k * 4096 + 512);
  {
    u32x4 v0, v1;
    int spin = SPIN_CAP;
    unsigned even = (lane & 1) - 1u;
    PIN(even);
    __builtin_amdgcn_s_sleep(PD);
#pragma unroll 1
    do {
      FENCE();
      v0 = __builtin_amdgcn_raw_buffer_load_b128(rw, psrc, 0, AUX_SC1);
      v1 = __builtin_amdgcn_raw_buffer_load_b128(rw, psrc + 16 * BOX, 0, AUX_SC1);
      const unsigned s0 = sum4(v0), s1 = sum4(v1);
      const unsigned c0 = s0 + rotr((unsigned)__builtin_amdgcn_mov_dpp((int)s0, XOR1, 0xf, 0xf, true), 5), c1 = s1 + rotr((unsigned)__builtin_amdgcn_mov_dpp((int)s1, XOR1, 0xf, 0xf, true), 5);
      if (__builtin_amdgcn_ballot_w64((((c0 ^ salt) | (c1 ^ salt)) & even) != 0) == 0) break;
      __builtin_amdgcn_s_sleep(PSL);
      GIVE_UP(even);
    } while (true);
#define LOWB(v) __uint_as_float(__builtin_amdgcn_perm(__builtin_amdgcn_perm(v[1], v[2], l0), __builtin_amdgcn_perm(v[3], v[0], l1), l2))
    const float e0 = dpp<ODD>(LOWB(v0)), e1 = dpp<ODD>(LOWB(v1));
    const float M = xrow_max(max3(max3(e0, e1, dpp<ROR8>(e0)), dpp<ROR8>(e1), e0));
    const float w0 = __builtin_amdgcn_exp2f(e0 - M), w1_ = __builtin_amdgcn_exp2f(e1 - M);
    float Ws = w0 + w1_; Ws += dpp<ROR8>(Ws); Ws = xrow_sum(Ws);
    float o = rows_reduce4(__builtin_elementwise_fma(vals(v1), f32x4{w1_, w1_, w1_, w1_}, vals(v0) * w0));
    o += dpp<ROR8>(o);
    o *= __builtin_amdgcn_rcpf(Ws);
    *(LAS u16*)lo = (u16)cvtpk(o, o);
  }
  BAR();
  if (wave < 4) {
    u32x4 ob[4];
#pragma unroll
    for (int k = 0; k < 4; ++k) ob[k] = *(LAS u32x4*)(lb + k * 64);
    SCHED();
    const f32x4 z4 = {0.f, 0.f, 0.f, 0.f};
    const f32x4 s = mfma16(wa[1], ob[1], mfma16(wa[0], ob[0], z4)) + mfma16(wa[3], ob[3], mfma16(wa[2], ob[2], z4));
    __builtin_amdgcn_raw_buffer_store_b128(seal(s, salt, k), rw, dst2, 0, AUX_SC1);
  }
  if (wave == 6 && j < 16) {
    const unsigned in = (j >> 2) * 128 + (lane & 1) * 64 + (j & 3) * 16;
    const unsigned zsrc = lane < 56 ? WS_BOX + (xcd * 32 + (lane >> 1)) * BOX + BX_ZB + in : OOB;
    const unsigned ssrc = WS_BOX + ((lane >> 3) * 32 + ((lane >> 1) & 3) * 8 + xcd) * BOX + BX_SB + in;
    u32x4 v;
    int spin = SPIN_CAP;
    unsigned need = lane < 56 ? ~0u : 0u;
#pragma unroll 1
    do {
      FENCE();
      v = __builtin_amdgcn_raw_buffer_load_b128(rw, zsrc, 0, AUX_SC1);
      if (__builtin_amdgcn_ballot_w64(((sum4(v) ^ salt) & need) != 0) == 0) break;
      GIVE_UP(need);
    } while (true);
    float z = rows_reduce4(vals(v));
    z += dpp<XOR2>(z); z += dpp<ROR4>(z); z += dpp<ROR8>(z);
    z = rbf(rbf(z) + bias2);
    float gate = rbf(z * __builtin_amdgcn_rcpf(1.f + __builtin_amdgcn_exp2f(-LOG2E * z)));
    unsigned odst = r < 2 ? (r * 512 + xcd * 64 + j * 4 + g) * 2 : OOB;
    PIN(gate); PIN(odst);
    spin = SPIN_CAP; need = ~0u;
#pragma unroll 1
    do {
      FENCE();
      v = __builtin_amdgcn_raw_buffer_load_b128(rw, ssrc, 0, AUX_SC1);
      if (__builtin_amdgcn_ballot_w64(((sum4(v) ^ salt) & need) != 0) == 0) break;
      GIVE_UP(need);
    } while (true);
    float s = rows_reduce4(vals(v));
    s += dpp<XOR2>(s); s += dpp<ROR4>(s); s += dpp<ROR8>(s);
    const float res = xres + rbf(rbf(s) * gate);
    const auto rout = __builtin_amdgcn_make_buffer_rsrc(out, 0, 2048, RS3);
    __builtin_amdgcn_raw_buffer_store_b16((u16)cvtpk(res, res), rout, odst, 0, 0);
    __builtin_amdgcn_raw_buffer_store_b64(u32x2{(unsigned)gnext, (unsigned)(gnext >> 32)}, rw, (blockIdx.x == 0 && lane == 0) ? WS_G : OOB, 0, AUX_SC1);
    asm volatile(".Lfd_last:");
  }
}

extern "C" void kernel_launch(void* const* d_in, const int* in_sizes, int n_in, void* d_out, int out_size, void* d_ws, size_t ws_size, hipStream_t stream) {
  fused_decode<<<256, 512, 0, stream>>>((const u16*)d_in[0], (const u16*)d_in[1], (const u16*)d_in[2], (const u16*)d_in[3], (const u16*)d_in[4],
                                        (const u16*)d_in[6], (const u16*)d_in[7], (const u16*)d_in[8], (const u16*)d_in[9], (u16*)d_out, (unsigned char*)d_ws);
}
```
